```python
import torch

def _softmax(x, axis=-1):
    return torch.softmax(x, dim=axis)
def _gelu(x):
    return torch.nn.functional.gelu(x, approximate="tanh")
def _silu(x):
    return torch.nn.functional.silu(x)
def _sigmoid(x):
    return torch.sigmoid(x)
def _layernorm(x, axis=-1, eps=1e-5):
    m = x.mean(dim=axis, keepdim=True)
    v = x.var(dim=axis, keepdim=True, unbiased=False)
    return (x - m) / torch.sqrt(v + eps)
def _scatter_add(tgt, idx, src, axis=0):
    return tgt.clone().index_add_(axis, idx, src)
def _segment_sum(src, seg, n):
    out = torch.zeros((n,) + src.shape[1:], dtype=src.dtype)
    return out.index_add_(0, seg, src)
def _pad(x, pw):
    flat = tuple(v for pair in reversed(pw) for v in pair)
    return torch.nn.functional.pad(x, flat)

kernel_name = 'standalone__gqa_softmax__near_ridge'

def setup_inputs():

    _w = (lambda a: (lambda t: t.to(torch.bfloat16) if t.is_floating_point() else t)(torch.as_tensor(a)))
    B, Hq, Hkv, Sq, Sk, D = 16, 32, 8, 496, 512, 128
    return {
        "q": _w((lambda *s: torch.randn(*s, dtype=torch.bfloat16) * 0.8)(B, Hq, Sq, D)),
        "k": _w((lambda *s: torch.randn(*s, dtype=torch.bfloat16) * 0.8)(B, Hkv, Sk, D)),
        "v": _w((lambda *s: torch.randn(*s, dtype=torch.bfloat16) * 0.05)(B, Hkv, Sk, D)),
    }

def reference(q, k, v):
    B, Hq, Sq, D = q.shape
    Hkv = k.shape[1]
    rep = Hq // Hkv
    k = (lambda x, n, axis: torch.repeat_interleave(x, n, dim=axis))(k, rep, axis=1)
    v = (lambda x, n, axis: torch.repeat_interleave(x, n, dim=axis))(v, rep, axis=1)
    scale = D ** -0.5
    scores = torch.einsum("bhqd,bhkd->bhqk", q, k) * scale
    p = _softmax(scores, axis=-1)
    return torch.einsum("bhqk,bhkd->bhqd", p, v)
```

```cpp
#include <hip/hip_runtime.h>
#include <hip/hip_bf16.h>
using bf16 = __hip_bfloat16;
constexpr int D = 128, NW = 8, QBLK = 32, KVBLK = 64;
constexpr float SCALE = 0.088388347648318440f;
constexpr float THR = 8.f;
constexpr int NKV = 128, MROWS = 1984, SKV = 512;
constexpr size_t SHM_V = KVBLK * D * 2, SHM_K = KVBLK * D * 2, SHM_ATTN = 2 * SHM_V + 2 * SHM_K + NW * 64 * 4;
using bf16x8 = __attribute__((ext_vector_type(8))) short;
using s16x4  = __attribute__((ext_vector_type(4))) short;
using f32x16 = __attribute__((ext_vector_type(16))) float;
using u32x4  = __attribute__((ext_vector_type(4))) unsigned;
#define KSWZ(row, colB) ((row) * 256 + ((colB) ^ (((row) & 7) << 4)))
#define SBAR() __builtin_amdgcn_sched_barrier(0)
__device__ __forceinline__ int crow(int r, int hi) { return (r & 3) + 8 * (r >> 2) + 4 * hi; }
__device__ __forceinline__ unsigned cvtpk(float lo, float hi) {
  unsigned r; asm volatile("v_cvt_pk_bf16_f32 %0, %1, %2" : "=v"(r) : "v"(lo), "v"(hi)); return r;
}
__device__ __forceinline__ bf16x8 ld8(const bf16* p) { return *reinterpret_cast<const bf16x8*>(p); }
__device__ __forceinline__ void partialSM(f32x16& p0, f32x16& p1, float& m_reg, float& mn, float& alpha) {
  constexpr float C = SCALE * 1.4426950408889634f;
  float pmax = p0[0]; for (int r = 1; r < 16; ++r) pmax = fmaxf(pmax, p0[r]); for (int r = 0; r < 16; ++r) pmax = fmaxf(pmax, p1[r]);
  { auto rr = __builtin_amdgcn_permlane32_swap(__float_as_uint(pmax), __float_as_uint(pmax), false, false);
    pmax = fmaxf(__uint_as_float(rr[0]), __uint_as_float(rr[1])); }
  if (__builtin_expect(__all(pmax - m_reg <= THR / SCALE), 1)) { mn = m_reg; alpha = 1.f; }
  else { mn = fmaxf(m_reg, pmax); alpha = __builtin_amdgcn_exp2f((m_reg - mn) * C); m_reg = mn; }
  float mnC = -mn * C;
  for (int r = 0; r < 16; ++r) p0[r] = fmaf(p0[r], C, mnC); for (int r = 0; r < 16; ++r) p1[r] = fmaf(p1[r], C, mnC);
  for (int r = 0; r < 16; ++r) p0[r] = __builtin_amdgcn_exp2f(p0[r]);
}
__device__ __forceinline__ void finishSM(f32x16& p0, f32x16& p1, float alpha, float& l_reg, bf16x8& pa0, bf16x8& pa1, bf16x8& pa2, bf16x8& pa3) {
  for (int r = 0; r < 16; ++r) p1[r] = __builtin_amdgcn_exp2f(p1[r]);
  float ps = 0; for (int r = 0; r < 16; ++r) ps += p0[r]; for (int r = 0; r < 16; ++r) ps += p1[r];
  { auto rr = __builtin_amdgcn_permlane32_swap(__float_as_uint(ps), __float_as_uint(ps), false, false);
    ps = __uint_as_float(rr[0]) + __uint_as_float(rr[1]); }
  l_reg = l_reg * alpha + ps;
#define PK4(P, BASE, OUT) do { unsigned a0 = cvtpk(P[BASE + 0], P[BASE + 1]), a1 = cvtpk(P[BASE + 2], P[BASE + 3]);   \
    unsigned b0 = cvtpk(P[BASE + 4], P[BASE + 5]), b1 = cvtpk(P[BASE + 6], P[BASE + 7]);                              \
    auto r0 = __builtin_amdgcn_permlane32_swap(a0, b0, false, false); auto r1 = __builtin_amdgcn_permlane32_swap(a1, b1, false, false); \
    u32x4 w = {r0[0], r1[0], r0[1], r1[1]}; OUT = *reinterpret_cast<bf16x8*>(&w); } while (0)
  PK4(p0, 0, pa0); PK4(p0, 8, pa1); PK4(p1, 0, pa2); PK4(p1, 8, pa3);
#undef PK4
}
__device__ __forceinline__ void qkt(f32x16& p0, f32x16& p1, const bf16* Ks, const bf16x8* qr, int r32, int hi) {
  p0 = f32x16{}; p1 = f32x16{};
  for (int d0 = 0; d0 < 8; ++d0) { int cb = (d0 * 16 + hi * 8) * 2;
    bf16x8 b0 = *reinterpret_cast<const bf16x8*>((const char*)Ks + KSWZ(r32, cb));
    bf16x8 b1 = *reinterpret_cast<const bf16x8*>((const char*)Ks + KSWZ(32 + r32, cb));
    p0 = __builtin_amdgcn_mfma_f32_32x32x16_bf16(b0, qr[d0], p0, 0, 0, 0);
    p1 = __builtin_amdgcn_mfma_f32_32x32x16_bf16(b1, qr[d0], p1, 0, 0, 0); }
}
__device__ __forceinline__ int v_st(int k, int c) { const int kk = (k & ~0xC) | ((k & 4) << 1) | ((k & 8) >> 1); return ((kk >> 3) * 4 + (c >> 5)) * 512 + ((kk & 7) * 32 + (c & 31)) * 2; }
__device__ __forceinline__ int v_rd_base(int lane) { return ((lane & 3) << 3) | (((lane >> 2) & 3) << 6) | (((lane >> 4) & 1) << 5) | (((lane >> 5) & 1) << 8); }
constexpr int v_rd_off(int d0, int ks, int half) { return d0 * 512 + ks * 4096 + half * 2048; }
template <int OFF> __device__ __forceinline__ s16x4 tr_read(int vb) {
  s16x4 r; asm volatile("ds_read_b64_tr_b16 %0, %1 offset:%2" : "=&v"(r) : "v"(vb), "i"(OFF) : "memory"); return r;
}
template <int D0> __device__ __forceinline__ void pv_one(f32x16& od, int vb, bf16x8 pa0, bf16x8 pa1, bf16x8 pa2, bf16x8 pa3) {
  const s16x4 l0 = tr_read<v_rd_off(D0, 0, 0)>(vb), h0 = tr_read<v_rd_off(D0, 0, 1)>(vb), l1 = tr_read<v_rd_off(D0, 1, 0)>(vb), h1 = tr_read<v_rd_off(D0, 1, 1)>(vb);
  const s16x4 l2 = tr_read<v_rd_off(D0, 2, 0)>(vb), h2 = tr_read<v_rd_off(D0, 2, 1)>(vb), l3 = tr_read<v_rd_off(D0, 3, 0)>(vb), h3 = tr_read<v_rd_off(D0, 3, 1)>(vb);
  asm volatile("s_waitcnt lgkmcnt(0)" ::: "memory"); SBAR();
#define PK(L, H) (bf16x8){L[0], L[1], L[2], L[3], H[0], H[1], H[2], H[3]}
  od = __builtin_amdgcn_mfma_f32_32x32x16_bf16(pa0, PK(l0, h0), od, 0, 0, 0);
  od = __builtin_amdgcn_mfma_f32_32x32x16_bf16(pa1, PK(l1, h1), od, 0, 0, 0);
  od = __builtin_amdgcn_mfma_f32_32x32x16_bf16(pa2, PK(l2, h2), od, 0, 0, 0);
  od = __builtin_amdgcn_mfma_f32_32x32x16_bf16(pa3, PK(l3, h3), od, 0, 0, 0);
#undef PK
}
__device__ __forceinline__ void pv_d0(f32x16* o, int vb, bf16x8 pa0, bf16x8 pa1, bf16x8 pa2, bf16x8 pa3) {
  pv_one<0>(o[0], vb, pa0, pa1, pa2, pa3); pv_one<1>(o[1], vb, pa0, pa1, pa2, pa3); pv_one<2>(o[2], vb, pa0, pa1, pa2, pa3); pv_one<3>(o[3], vb, pa0, pa1, pa2, pa3);
}
__global__ __launch_bounds__(64 * NW, 1)
void attn_fwd(const bf16* __restrict__ Q, const bf16* __restrict__ K, const bf16* __restrict__ V, bf16* __restrict__ O) {
  extern __shared__ __attribute__((aligned(16))) char lds[];
  const int id = blockIdx.x; const int kv = (id & 7) + 8 * (id >> 6); const int qb = (id >> 3) & 7;
  const int tid = threadIdx.x, wid = tid >> 6, lane = tid & 63, r32 = lane & 31, hi = lane >> 5;
  const bf16* Kh = K + (long)kv * SKV * D; const bf16* Vh = V + (long)kv * SKV * D;
  const int row0 = qb * 256 + wid * QBLK; const bool valid = row0 < MROWS;
  const int rowc = valid ? row0 : 0;
  constexpr int LDK = D; const int seq = SKV;
  bf16* V_lds = (bf16*)lds; bf16* K_lds = (bf16*)(lds + 2 * SHM_V);
  float* ws = (float*)(lds + 2 * SHM_V + 2 * SHM_K) + wid * 64; float* li_l = ws; float* al_l = ws + 32;
  float m_reg = -1e30f, l_reg = 0; f32x16 o[4] = {}; bf16x8 qr[8];
  const bf16* Qw = Q + ((long)kv * MROWS + rowc + r32) * D + hi * 8;
#pragma unroll
  for (int d0 = 0; d0 < 8; ++d0) qr[d0] = ld8(Qw + d0 * 16);
  const int sr = tid >> 4, sc = (tid & 15) * 8, vst0 = v_st(sr, sc), vst1 = v_st(32 + sr, sc);
  const int vb0 = (int)(uintptr_t)V_lds + v_rd_base(lane);
  struct { bf16x8 vs0, vs1, ks0, ks1; } sr_[2];
#define SLOAD(i, k0) do { sr_[i].vs0 = ld8(&Vh[(long)((k0) + sr) * LDK + sc]); sr_[i].vs1 = ld8(&Vh[(long)((k0) + 32 + sr) * LDK + sc]); \
    sr_[i].ks0 = ld8(&Kh[(long)((k0) + sr) * LDK + sc]); sr_[i].ks1 = ld8(&Kh[(long)((k0) + 32 + sr) * LDK + sc]); } while (0)
#define SWRITE(b, i) do { *(bf16x8*)((char*)V_lds + (b) * SHM_V + vst0) = sr_[i].vs0;          \
    *(bf16x8*)((char*)V_lds + (b) * SHM_V + vst1) = sr_[i].vs1; int kc = sc * 2;               \
    *(bf16x8*)((char*)K_lds + (b) * SHM_K + KSWZ(sr, kc)) = sr_[i].ks0;                       \
    *(bf16x8*)((char*)K_lds + (b) * SHM_K + KSWZ(32 + sr, kc)) = sr_[i].ks1; } while (0)
#define SWAIT() do { asm volatile("s_waitcnt vmcnt(4)" ::: "memory"); } while (0)
#define RESC(a) do { if (__any((a) < 1.f)) { if (hi == 0) al_l[r32] = (a); asm volatile("s_waitcnt lgkmcnt(0)" ::: "memory"); \
    for (int d = 0; d < 4; ++d) for (int r = 0; r < 16; ++r) o[d][r] *= al_l[crow(r, hi)]; } } while (0)
  f32x16 pA0, pA1, pB0, pB1; float mnA, mnB, alA, alB; bf16x8 pa0, pa1, pa2, pa3; const int NT = seq / KVBLK;
  constexpr int SE = 0, SO = 1, SDEPTH = 2;
  SLOAD(SE, 0); asm volatile("s_waitcnt vmcnt(0)" ::: "memory"); SWRITE(0, SE); __syncthreads();
  qkt(pA0, pA1, K_lds, qr, r32, hi); partialSM(pA0, pA1, m_reg, mnA, alA);
  SLOAD(SO, KVBLK); SLOAD(SE, 2 * KVBLK);
  SWAIT(); SWRITE(1, SO); __syncthreads();
  for (int j = 1; j + 1 < NT; j += 2) {
    SBAR(); qkt(pB0, pB1, (bf16*)((char*)K_lds + SHM_K), qr, r32, hi);
    finishSM(pA0, pA1, alA, l_reg, pa0, pa1, pa2, pa3); SBAR();
    SLOAD(SO, (j + SDEPTH) * KVBLK); SBAR();
    pv_d0(o, vb0, pa0, pa1, pa2, pa3); partialSM(pB0, pB1, m_reg, mnB, alB);
    __syncthreads(); SWAIT(); SWRITE(0, SE);
    RESC(alB); __syncthreads();
    SBAR(); qkt(pA0, pA1, K_lds, qr, r32, hi);
    finishSM(pB0, pB1, alB, l_reg, pa0, pa1, pa2, pa3); SBAR();
    if (j + 3 < NT) SLOAD(SE, (j + 1 + SDEPTH) * KVBLK); SBAR();
    pv_d0(o, vb0 + (int)SHM_V, pa0, pa1, pa2, pa3); partialSM(pA0, pA1, m_reg, mnA, alA);
    __syncthreads(); if (j + 3 < NT) SWAIT(); else asm volatile("s_waitcnt vmcnt(0)" ::: "memory"); SWRITE(1, SO);
    RESC(alA); __syncthreads();
  }
  SBAR(); qkt(pB0, pB1, (bf16*)((char*)K_lds + SHM_K), qr, r32, hi);
  finishSM(pA0, pA1, alA, l_reg, pa0, pa1, pa2, pa3); SBAR();
  pv_d0(o, vb0, pa0, pa1, pa2, pa3); partialSM(pB0, pB1, m_reg, mnB, alB);
  __syncthreads(); RESC(alB);
  finishSM(pB0, pB1, alB, l_reg, pa0, pa1, pa2, pa3); SBAR();
  pv_d0(o, vb0 + (int)SHM_V, pa0, pa1, pa2, pa3);
  if (hi == 0) li_l[r32] = l_reg; asm volatile("s_waitcnt lgkmcnt(0)" ::: "memory");
  float rli[16];
#pragma unroll
  for (int r = 0; r < 16; ++r) rli[r] = __builtin_amdgcn_rcpf(li_l[crow(r, hi)]);
  if (valid) {
    bf16* Ow = O + ((long)kv * MROWS + row0) * D;
#pragma unroll
    for (int r = 0; r < 16; ++r) { int orow = crow(r, hi);
      for (int d0 = 0; d0 < 4; ++d0) Ow[(long)orow * D + d0 * 32 + r32] = __float2bfloat16(o[d0][r] * rli[r]); }
  }
}
extern "C" void kernel_launch(void* const* d_in, const int* in_sizes, int n_in, void* d_out, int out_size, void* d_ws, size_t ws_size, hipStream_t stream) {
  (void)hipFuncSetAttribute((const void*)attn_fwd, hipFuncAttributeMaxDynamicSharedMemorySize, (int)SHM_ATTN);
  hipLaunchKernelGGL(attn_fwd, dim3(NKV * 8), dim3(64 * NW), SHM_ATTN, stream,
                     (const bf16*)d_in[0], (const bf16*)d_in[1], (const bf16*)d_in[2], (bf16*)d_out);
}
```

```cpp
#include <hip/hip_runtime.h>
#include <hip/hip_bf16.h>

constexpr int kLdsBytes = 163840;

__global__ __launch_bounds__(256, 1) void gqa_softmax_attn_kernel(const char* __restrict__ Q, const char* __restrict__ K,
                                                                  const char* __restrict__ V, char* __restrict__ O) {
  __shared__ __attribute__((aligned(1024))) char lds[kLdsBytes];
  const unsigned bid = blockIdx.x;
  const unsigned kv = (bid & 7u) + 8u * (bid >> 4);
  const unsigned half = (bid >> 3) & 1u;
  const unsigned wid = __builtin_amdgcn_readfirstlane(threadIdx.x >> 6);
  const unsigned long long qoff = ((unsigned long long)kv * 1984u + half * 992u) * 256u;
  const unsigned long long q = (unsigned long long)Q + qoff, o = (unsigned long long)O + qoff;
  const unsigned long long k = (unsigned long long)K + (unsigned long long)kv * 131072u, v = (unsigned long long)V + (unsigned long long)kv * 131072u;
  const unsigned ldsb = __builtin_amdgcn_readfirstlane((unsigned)(size_t)lds);
  asm volatile(
    "s_mov_b32 s67, %8\n"
    "s_mov_b32 s44, %2\n"
    "s_and_b32 s45, %3, 0xffff\n"
    "s_mov_b32 s46, 131072\n"
    "s_mov_b32 s47, 0x20000\n"
    "v_mbcnt_lo_u32_b32 v233, -1, 0\n"
    "v_mbcnt_hi_u32_b32 v233, -1, v233\n"
    "v_bfe_u32 v234, v233, 2, 2\n"
    "v_lshlrev_b32 v231, 8, v234\n"
    "v_lshrrev_b32 v234, 4, v233\n"
    "v_lshl_add_u32 v231, v234, 6, v231\n"
    "v_and_b32 v235, 3, v233\n"
    "v_lshl_add_u32 v231, v235, 4, v231\n"
    "s_mov_b32 s72, %10\n"
    "s_xor_b32 s69, s72, 0\n"
    "buffer_load_dwordx4 v[48:51], v231, s[44:47], s69 offen offset:0\n"
    "buffer_load_dwordx4 v[52:55], v231, s[44:47], s69 offen offset:1024\n"
    "buffer_load_dwordx4 v[56:59], v231, s[44:47], s69 offen offset:2048\n"
    "buffer_load_dwordx4 v[60:63], v231, s[44:47], s69 offen offset:3072\n"
    "s_xor_b32 s69, s72, 4096\n"
    "buffer_load_dwordx4 v[64:67], v231, s[44:47], s69 offen offset:0\n"
    "buffer_load_dwordx4 v[68:71], v231, s[44:47], s69 offen offset:1024\n"
    "buffer_load_dwordx4 v[72:75], v231, s[44:47], s69 offen offset:2048\n"
    "buffer_load_dwordx4 v[76:79], v231, s[44:47], s69 offen offset:3072\n"
    "s_mov_b32 s71, %9\n"
    "s_mov_b32 s40, %0\n"
    "s_and_b32 s41, %1, 0xffff\n"
    "s_mov_b32 s42, 253952\n"
    "s_mov_b32 s43, 0x20000\n"
    "s_mul_i32 s68, s67, 2080\n"
    "s_and_b32 s68, s68, 0x1820\n"
    "v_xor_b32 v228, s68, v231\n"
    "s_lshl_b32 s66, s67, 14\n"
    "s_add_u32 s66, s66, s71\n"
    "s_add_u32 s66, s66, 73728\n"
    "s_xor_b32 m0, s66, 8192\n"
    "s_mov_b32 s68, 0\n"
    "buffer_load_dwordx4 v228, s[40:43], s68 offen offset:0 lds\n"
    "buffer_load_dwordx4 v228, s[40:43], s68 offen offset:1024 lds\n"
    "s_xor_b32 m0, s66, 10240\n"
    "s_mov_b32 s68, 8192\n"
    "buffer_load_dwordx4 v228, s[40:43], s68 offen offset:0 lds\n"
    "buffer_load_dwordx4 v228, s[40:43], s68 offen offset:1024 lds\n"
    "s_mov_b32 s48, %4\n"
    "s_and_b32 s49, %5, 0xffff\n"
    "s_mov_b32 s50, 131072\n"
    "s_mov_b32 s51, 0x20000\n"
    "s_add_u32 s36, %0, 40960\n"
    "s_addc_u32 s37, %1, 0\n"
    "s_and_b32 s37, s37, 0xffff\n"
    "s_mov_b32 s38, 212992\n"
    "s_mov_b32 s39, 0x20000\n"
    "s_sub_u32 s52, %6, 16384\n"
    "s_subb_u32 s53, %7, 0\n"
    "s_and_b32 s53, s53, 0xffff\n"
    "s_mov_b32 s54, 270336\n"
    "s_mov_b32 s55, 0x20000\n"
    "s_mov_b32 s60, 0x3e0293ee\n"
    "s_mov_b32 s61, 0x3e0293ee\n"
    "s_mov_b32 s62, 0xffff0000\n"
    "s_mov_b32 s63, 0x33800000\n"
    "s_mov_b32 s64, 0x20000000\n"
    "s_mov_b32 s57, 0\n"
    "s_mov_b32 s56, 16384\n"
    "v_and_b32 v218, 15, v233\n"
    "v_lshlrev_b32 v229, 8, v218\n"
    "v_lshl_add_u32 v229, v234, 4, v229\n"
    "s_lshl_b32 s68, s67, 6\n"
    "v_add_u32 v229, s68, v229\n"
    "v_add_u32 v230, 0x1000, v229\n"
    "v_and_b32 v219, 7, v233\n"
    "s_mov_b32 s68, 0x3f803f80\n"
    "v_mov_b32 v212, s68\n"
    "v_cmp_eq_u32 vcc, 0, v219\n"
    "s_nop 1\n"
    "v_cndmask_b32 v204, 0, v212, vcc\n"
    "v_cndmask_b32 v205, 0, v212, vcc\n"
    "v_cndmask_b32 v206, 0, v212, vcc\n"
    "v_cndmask_b32 v207, 0, v212, vcc\n"
    "v_cmp_eq_u32 vcc, 4, v219\n"
    "s_nop 1\n"
    "v_cndmask_b32 v208, 0, v212, vcc\n"
    "v_cndmask_b32 v209, 0, v212, vcc\n"
    "v_cndmask_b32 v210, 0, v212, vcc\n"
    "v_cndmask_b32 v211, 0, v212, vcc\n"
    "v_lshlrev_b32 v219, 6, v235\n"
    "v_lshl_add_u32 v219, v234, 4, v219\n"
    "v_bfe_u32 v218, v233, 2, 1\n"
    "v_lshl_add_u32 v219, v218, 10, v219\n"
    "v_bfe_u32 v218, v233, 3, 1\n"
    "v_lshlrev_b32 v232, 5, v218\n"
    "v_xor_b32 v219, v219, v232\n"
    "s_add_u32 s68, s71, 65536\n"
    "v_lshl_add_u32 v221, v218, 14, v219\n"
    "v_add_u32 v221, s68, v221\n"
    "v_lshl_add_u32 v232, v218, 11, v219\n"
    "s_lshl_b32 s81, s67, 13\n"
    "s_add_u32 s81, s81, s71\n"
    "s_add_u32 s81, s81, 131072\n"
    "s_sub_u32 s68, s66, 4096\n"
    "v_add_u32 v232, s68, v232\n"
    "v_lshl_add_u32 v240, v233, 4, s68\n"
    "v_xor_b32 v241, 32, v240\n"
    "v_and_b32 v218, 31, v233\n"
    "s_add_u32 s68, s71, 131072\n"
    "v_lshl_add_u32 v227, v218, 4, s68\n"
    "s_lshl_b32 s68, s67, 2\n"
    "v_add_u32 v226, s68, v227\n"
    "v_subrev_u32 v243, 59392, v226\n"
    "v_subrev_u32 v244, 59392, v227\n"
    "v_lshlrev_b32 v219, 10, v234\n"
    "v_bfe_u32 v218, v233, 2, 2\n"
    "v_lshl_add_u32 v219, v218, 6, v219\n"
    "v_lshl_add_u32 v219, v235, 4, v219\n"
    "s_lshl_b32 s68, s67, 8\n"
    "s_add_u32 s68, s68, s71\n"
    "s_add_u32 s68, s68, 131072\n"
    "v_add_u32 v219, s68, v219\n"
    "v_lshlrev_b32 v218, 4, v233\n"
    "s_add_u32 s68, s67, 0\n"
    "s_and_b32 s68, s68, 3\n"
    "s_lshl_b32 s68, s68, 13\n"
    "v_add_u32 v236, s68, v219\n"
    "s_lshl_b32 s68, s68, 1\n"
    "s_sub_u32 s65, s68, 65536\n"
    "v_add_u32 v248, s65, v219\n"
    "s_add_u32 s68, s68, s71\n"
    "v_add_u32 v222, s68, v218\n"
    "s_add_u32 s68, s67, 1\n"
    "s_and_b32 s68, s68, 3\n"
    "s_lshl_b32 s68, s68, 13\n"
    "v_add_u32 v237, s68, v219\n"
    "s_lshl_b32 s68, s68, 1\n"
    "s_sub_u32 s65, s68, 65536\n"
    "v_add_u32 v249, s65, v219\n"
    "s_add_u32 s68, s68, s71\n"
    "v_add_u32 v223, s68, v218\n"
    "s_add_u32 s68, s67, 2\n"
    "s_and_b32 s68, s68, 3\n"
    "s_lshl_b32 s68, s68, 13\n"
    "v_add_u32 v238, s68, v219\n"
    "s_lshl_b32 s68, s68, 1\n"
    "s_sub_u32 s65, s68, 65536\n"
    "v_add_u32 v250, s65, v219\n"
    "s_add_u32 s68, s68, s71\n"
    "v_add_u32 v224, s68, v218\n"
    "s_add_u32 s68, s67, 3\n"
    "s_and_b32 s68, s68, 3\n"
    "s_lshl_b32 s68, s68, 13\n"
    "v_add_u32 v239, s68, v219\n"
    "s_lshl_b32 s68, s68, 1\n"
    "s_sub_u32 s65, s68, 65536\n"
    "v_add_u32 v251, s65, v219\n"
    "s_add_u32 s68, s68, s71\n"
    "v_add_u32 v225, s68, v218\n"
    "s_mov_b32 s59, 0x2000\n"
    "s_xor_b32 s58, s59, s66\n"
    "v_add_u32 v220, 0x1000, v221\n"
    "s_waitcnt vmcnt(10) lgkmcnt(0)\n"
    "s_xor_b32 s69, s72, 8192\n"
    "buffer_load_dwordx4 v[112:115], v231, s[44:47], s69 offen offset:0\n"
    "v_lshlrev_b32 v176, 16, v48\n"
    "v_and_b32 v177, s62, v48\n"
    "v_lshlrev_b32 v178, 16, v49\n"
    "v_and_b32 v179, s62, v49\n"
    "v_lshlrev_b32 v180, 16, v50\n"
    "v_and_b32 v181, s62, v50\n"
    "v_lshlrev_b32 v182, 16, v51\n"
    "v_and_b32 v183, s62, v51\n"
    "v_pk_mul_f32 v[176:177], v[176:177], s[60:61]\n"
    "v_pk_mul_f32 v[178:179], v[178:179], s[60:61]\n"
    "v_pk_mul_f32 v[180:181], v[180:181], s[60:61]\n"
    "v_pk_mul_f32 v[182:183], v[182:183], s[60:61]\n"
    "v_cvt_pk_bf16_f32 v48, v176, v177\n"
    "v_cvt_pk_bf16_f32 v49, v178, v179\n"
    "v_cvt_pk_bf16_f32 v50, v180, v181\n"
    "v_cvt_pk_bf16_f32 v51, v182, v183\n"
    "ds_write_b128 v240, v[48:51] offset:0\n"
    "v_lshlrev_b32 v184, 16, v52\n"
    "buffer_load_dwordx4 v[116:119], v231, s[44:47], s69 offen offset:1024\n"
    "v_and_b32 v185, s62, v52\n"
    "v_lshlrev_b32 v186, 16, v53\n"
    "v_and_b32 v187, s62, v53\n"
    "v_lshlrev_b32 v188, 16, v54\n"
    "v_and_b32 v189, s62, v54\n"
    "v_lshlrev_b32 v190, 16, v55\n"
    "v_and_b32 v191, s62, v55\n"
    "v_pk_mul_f32 v[184:185], v[184:185], s[60:61]\n"
    "v_pk_mul_f32 v[186:187], v[186:187], s[60:61]\n"
    "v_pk_mul_f32 v[188:189], v[188:189], s[60:61]\n"
    "v_pk_mul_f32 v[190:191], v[190:191], s[60:61]\n"
    "v_cvt_pk_bf16_f32 v52, v184, v185\n"
    "v_cvt_pk_bf16_f32 v53, v186, v187\n"
    "v_cvt_pk_bf16_f32 v54, v188, v189\n"
    "v_cvt_pk_bf16_f32 v55, v190, v191\n"
    "ds_write_b128 v240, v[52:55] offset:1024\n"
    "s_waitcnt vmcnt(10)\n"
    "v_lshlrev_b32 v192, 16, v56\n"
    "buffer_load_dwordx4 v[120:123], v231, s[44:47], s69 offen offset:2048\n"
    "v_and_b32 v193, s62, v56\n"
    "v_lshlrev_b32 v194, 16, v57\n"
    "v_and_b32 v195, s62, v57\n"
    "v_lshlrev_b32 v196, 16, v58\n"
    "v_and_b32 v197, s62, v58\n"
    "v_lshlrev_b32 v198, 16, v59\n"
    "v_and_b32 v199, s62, v59\n"
    "v_pk_mul_f32 v[192:193], v[192:193], s[60:61]\n"
    "v_pk_mul_f32 v[194:195], v[194:195], s[60:61]\n"
    "v_pk_mul_f32 v[196:197], v[196:197], s[60:61]\n"
    "v_pk_mul_f32 v[198:199], v[198:199], s[60:61]\n"
    "v_cvt_pk_bf16_f32 v56, v192, v193\n"
    "v_cvt_pk_bf16_f32 v57, v194, v195\n"
    "v_cvt_pk_bf16_f32 v58, v196, v197\n"
    "v_cvt_pk_bf16_f32 v59, v198, v199\n"
    "ds_write_b128 v241, v[56:59] offset:2048\n"
    "v_lshlrev_b32 v176, 16, v60\n"
    "v_and_b32 v177, s62, v60\n"
    "v_lshlrev_b32 v178, 16, v61\n"
    "buffer_load_dwordx4 v[124:127], v231, s[44:47], s69 offen offset:3072\n"
    "v_and_b32 v179, s62, v61\n"
    "v_lshlrev_b32 v180, 16, v62\n"
    "v_and_b32 v181, s62, v62\n"
    "v_lshlrev_b32 v182, 16, v63\n"
    "v_and_b32 v183, s62, v63\n"
    "v_pk_mul_f32 v[176:177], v[176:177], s[60:61]\n"
    "v_pk_mul_f32 v[178:179], v[178:179], s[60:61]\n"
    "v_pk_mul_f32 v[180:181], v[180:181], s[60:61]\n"
    "v_pk_mul_f32 v[182:183], v[182:183], s[60:61]\n"
    "v_cvt_pk_bf16_f32 v60, v176, v177\n"
    "v_cvt_pk_bf16_f32 v61, v178, v179\n"
    "v_cvt_pk_bf16_f32 v62, v180, v181\n"
    "v_cvt_pk_bf16_f32 v63, v182, v183\n"
    "ds_write_b128 v241, v[60:63] offset:3072\n"
    "ds_read_b128 a[0:3], v232 offset:0\n"
    "ds_read_b128 a[4:7], v232 offset:256\n"
    "ds_read_b128 a[8:11], v232 offset:512\n"
    "ds_read_b128 a[12:15], v232 offset:768\n"
    "s_xor_b32 s69, s72, 12288\n"
    "buffer_load_dwordx4 v[128:131], v231, s[44:47], s69 offen offset:0\n"
    "s_waitcnt vmcnt(11)\n"
    "v_lshlrev_b32 v184, 16, v64\n"
    "v_and_b32 v185, s62, v64\n"
    "v_lshlrev_b32 v186, 16, v65\n"
    "v_and_b32 v187, s62, v65\n"
    "v_lshlrev_b32 v188, 16, v66\n"
    "v_and_b32 v189, s62, v66\n"
    "v_lshlrev_b32 v190, 16, v67\n"
    "v_and_b32 v191, s62, v67\n"
    "v_pk_mul_f32 v[184:185], v[184:185], s[60:61]\n"
    "v_pk_mul_f32 v[186:187], v[186:187], s[60:61]\n"
    "v_pk_mul_f32 v[188:189], v[188:189], s[60:61]\n"
    "v_pk_mul_f32 v[190:191], v[190:191], s[60:61]\n"
    "v_cvt_pk_bf16_f32 v64, v184, v185\n"
    "v_cvt_pk_bf16_f32 v65, v186, v187\n"
    "v_cvt_pk_bf16_f32 v66, v188, v189\n"
    "v_cvt_pk_bf16_f32 v67, v190, v191\n"
    "ds_write_b128 v240, v[64:67] offset:0\n"
    "buffer_load_dwordx4 v[132:135], v231, s[44:47], s69 offen offset:1024\n"
    "v_lshlrev_b32 v192, 16, v68\n"
    "v_and_b32 v193, s62, v68\n"
    "v_lshlrev_b32 v194, 16, v69\n"
    "v_and_b32 v195, s62, v69\n"
    "v_lshlrev_b32 v196, 16, v70\n"
    "v_and_b32 v197, s62, v70\n"
    "v_lshlrev_b32 v198, 16, v71\n"
    "v_and_b32 v199, s62, v71\n"
    "v_pk_mul_f32 v[192:193], v[192:193], s[60:61]\n"
    "v_pk_mul_f32 v[194:195], v[194:195], s[60:61]\n"
    "v_pk_mul_f32 v[196:197], v[196:197], s[60:61]\n"
    "v_pk_mul_f32 v[198:199], v[198:199], s[60:61]\n"
    "v_cvt_pk_bf16_f32 v68, v192, v193\n"
    "v_cvt_pk_bf16_f32 v69, v194, v195\n"
    "v_cvt_pk_bf16_f32 v70, v196, v197\n"
    "v_cvt_pk_bf16_f32 v71, v198, v199\n"
    "ds_write_b128 v240, v[68:71] offset:1024\n"
    "s_waitcnt vmcnt(10)\n"
    "v_lshlrev_b32 v176, 16, v72\n"
    "buffer_load_dwordx4 v[136:139], v231, s[44:47], s69 offen offset:2048\n"
    "v_and_b32 v177, s62, v72\n"
    "v_lshlrev_b32 v178, 16, v73\n"
    "v_and_b32 v179, s62, v73\n"
    "v_lshlrev_b32 v180, 16, v74\n"
    "v_and_b32 v181, s62, v74\n"
    "v_lshlrev_b32 v182, 16, v75\n"
    "v_and_b32 v183, s62, v75\n"
    "v_pk_mul_f32 v[176:177], v[176:177], s[60:61]\n"
    "v_pk_mul_f32 v[178:179], v[178:179], s[60:61]\n"
    "v_pk_mul_f32 v[180:181], v[180:181], s[60:61]\n"
    "v_pk_mul_f32 v[182:183], v[182:183], s[60:61]\n"
    "v_cvt_pk_bf16_f32 v72, v176, v177\n"
    "v_cvt_pk_bf16_f32 v73, v178, v179\n"
    "v_cvt_pk_bf16_f32 v74, v180, v181\n"
    "v_cvt_pk_bf16_f32 v75, v182, v183\n"
    "ds_write_b128 v241, v[72:75] offset:2048\n"
    "v_lshlrev_b32 v184, 16, v76\n"
    "v_and_b32 v185, s62, v76\n"
    "buffer_load_dwordx4 v[140:143], v231, s[44:47], s69 offen offset:3072\n"
    "v_lshlrev_b32 v186, 16, v77\n"
    "v_and_b32 v187, s62, v77\n"
    "v_lshlrev_b32 v188, 16, v78\n"
    "v_and_b32 v189, s62, v78\n"
    "v_lshlrev_b32 v190, 16, v79\n"
    "v_and_b32 v191, s62, v79\n"
    "v_pk_mul_f32 v[184:185], v[184:185], s[60:61]\n"
    "v_pk_mul_f32 v[186:187], v[186:187], s[60:61]\n"
    "v_pk_mul_f32 v[188:189], v[188:189], s[60:61]\n"
    "v_pk_mul_f32 v[190:191], v[190:191], s[60:61]\n"
    "v_cvt_pk_bf16_f32 v76, v184, v185\n"
    "v_cvt_pk_bf16_f32 v77, v186, v187\n"
    "v_cvt_pk_bf16_f32 v78, v188, v189\n"
    "v_cvt_pk_bf16_f32 v79, v190, v191\n"
    "ds_write_b128 v241, v[76:79] offset:3072\n"
    "ds_read_b128 a[16:19], v232 offset:0\n"
    "ds_read_b128 a[20:23], v232 offset:256\n"
    "ds_read_b128 a[24:27], v232 offset:512\n"
    "s_waitcnt lgkmcnt(4)\n"
    "ds_read_b128 a[28:31], v232 offset:768\n"
    "s_waitcnt vmcnt(10) lgkmcnt(4)\n"
    "s_barrier\n"
    "ds_read_b128 v[16:19], v221 offset:0\n"
    "ds_read_b128 v[20:23], v221 offset:256\n"
    "ds_read_b128 v[24:27], v221 offset:512\n"
    "ds_read_b128 v[28:31], v221 offset:768\n"
    "ds_read_b128 v[32:35], v221 offset:32768\n"
    "ds_read_b128 v[36:39], v221 offset:33024\n"
    "ds_read_b128 v[40:43], v221 offset:33280\n"
    "ds_read_b128 v[44:47], v221 offset:33536\n"
    "s_waitcnt lgkmcnt(7)\n"
    "v_mfma_f32_16x16x32_bf16 v[0:3], a[0:3], v[16:19], -4.0\n"
    "s_waitcnt lgkmcnt(6)\n"
    "v_mfma_f32_16x16x32_bf16 v[0:3], a[4:7], v[20:23], v[0:3]\n"
    "s_waitcnt lgkmcnt(5)\n"
    "v_mfma_f32_16x16x32_bf16 v[0:3], a[8:11], v[24:27], v[0:3]\n"
    "s_waitcnt lgkmcnt(4)\n"
    "v_mfma_f32_16x16x32_bf16 v[0:3], a[12:15], v[28:31], v[0:3]\n"
    "s_waitcnt lgkmcnt(3)\n"
    "v_mfma_f32_16x16x32_bf16 v[4:7], a[0:3], v[32:35], -4.0\n"
    "s_waitcnt lgkmcnt(2)\n"
    "v_mfma_f32_16x16x32_bf16 v[4:7], a[4:7], v[36:39], v[4:7]\n"
    "s_waitcnt lgkmcnt(1)\n"
    "v_mfma_f32_16x16x32_bf16 v[4:7], a[8:11], v[40:43], v[4:7]\n"
    "s_waitcnt lgkmcnt(0)\n"
    "v_mfma_f32_16x16x32_bf16 v[4:7], a[12:15], v[44:47], v[4:7]\n"
    "v_exp_f32 v0, v0\n"
    "v_exp_f32 v1, v1\n"
    "v_exp_f32 v2, v2\n"
    "v_cvt_pk_bf16_f32 v48, v0, v1\n"
    "v_exp_f32 v3, v3\n"
    "v_mfma_f32_16x16x32_bf16 v[8:11], a[16:19], v[16:19], -4.0\n"
    "v_cvt_pk_bf16_f32 v49, v2, v3\n"
    "s_nop 0\n"
    "v_exp_f32 v4, v4\n"
    "v_mfma_f32_16x16x32_bf16 v[8:11], a[20:23], v[20:23], v[8:11]\n"
    "v_exp_f32 v5, v5\n"
    "v_mfma_f32_16x16x32_bf16 v[8:11], a[24:27], v[24:27], v[8:11]\n"
    "v_exp_f32 v6, v6\n"
    "v_cvt_pk_bf16_f32 v64, v4, v5\n"
    "v_mfma_f32_16x16x32_bf16 v[8:11], a[28:31], v[28:31], v[8:11]\n"
    "v_exp_f32 v7, v7\n"
    "s_waitcnt vmcnt(8) lgkmcnt(0)\n"
    "s_barrier\n"
    "s_xor_b32 s69, s72, 16384\n"
    "buffer_load_dwordx4 v[144:147], v231, s[44:47], s69 offen offset:0\n"
    "v_mfma_f32_16x16x32_bf16 v[12:15], a[16:19], v[32:35], -4.0\n"
    "ds_read_b128 v[16:19], v221 offset:2048\n"
    "v_cvt_pk_bf16_f32 v65, v6, v7\n"
    "v_mfma_f32_16x16x32_bf16 v[12:15], a[20:23], v[36:39], v[12:15]\n"
    "ds_read_b128 v[20:23], v221 offset:2304\n"
    "v_mfma_f32_16x16x32_bf16 v[12:15], a[24:27], v[40:43], v[12:15]\n"
    "ds_read_b128 v[24:27], v221 offset:2560\n"
    "v_mfma_f32_16x16x32_bf16 v[12:15], a[28:31], v[44:47], v[12:15]\n"
    "ds_read_b128 v[28:31], v221 offset:2816\n"
    "ds_read_b128 v[32:35], v221 offset:34816\n"
    "ds_read_b128 v[36:39], v221 offset:35072\n"
    "ds_read_b128 v[40:43], v221 offset:35328\n"
    "ds_read_b128 v[44:47], v221 offset:35584\n"
    "buffer_load_dwordx4 v[148:151], v231, s[44:47], s69 offen offset:1024\n"
    "v_exp_f32 v8, v8\n"
    "v_exp_f32 v9, v9\n"
    "v_exp_f32 v10, v10\n"
    "v_cvt_pk_bf16_f32 v50, v8, v9\n"
    "v_exp_f32 v11, v11\n"
    "s_waitcnt lgkmcnt(6)\n"
    "v_mfma_f32_16x16x32_bf16 v[0:3], a[0:3], v[16:19], -4.0\n"
    "v_cvt_pk_bf16_f32 v51, v10, v11\n"
    "v_exp_f32 v12, v12\n"
    "v_mfma_f32_16x16x32_bf16 v[0:3], a[4:7], v[20:23], v[0:3]\n"
    "ds_write_b128 v222, v[48:51] offset:0\n"
    "v_exp_f32 v13, v13\n"
    "s_waitcnt lgkmcnt(1)\n"
    "v_mfma_f32_16x16x32_bf16 v[0:3], a[8:11], v[24:27], v[0:3]\n"
    "v_exp_f32 v14, v14\n"
    "v_cvt_pk_bf16_f32 v66, v12, v13\n"
    "buffer_load_dwordx4 v[152:155], v231, s[44:47], s69 offen offset:2048\n"
    "v_mfma_f32_16x16x32_bf16 v[0:3], a[12:15], v[28:31], v[0:3]\n"
    "v_exp_f32 v15, v15\n"
    "v_mfma_f32_16x16x32_bf16 v[4:7], a[16:19], v[16:19], -4.0\n"
    "v_cvt_pk_bf16_f32 v67, v14, v15\n"
    "v_mfma_f32_16x16x32_bf16 v[4:7], a[20:23], v[20:23], v[4:7]\n"
    "ds_write_b128 v222, v[64:67] offset:4096\n"
    "v_mfma_f32_16x16x32_bf16 v[4:7], a[24:27], v[24:27], v[4:7]\n"
    "v_mfma_f32_16x16x32_bf16 v[4:7], a[28:31], v[28:31], v[4:7]\n"
    "v_mfma_f32_16x16x32_bf16 v[8:11], a[0:3], v[32:35], -4.0\n"
    "v_exp_f32 v0, v0\n"
    "v_mfma_f32_16x16x32_bf16 v[8:11], a[4:7], v[36:39], v[8:11]\n"
    "v_exp_f32 v1, v1\n"
    "v_mfma_f32_16x16x32_bf16 v[8:11], a[8:11], v[40:43], v[8:11]\n"
    "v_exp_f32 v2, v2\n"
    "buffer_load_dwordx4 v[156:159], v231, s[44:47], s69 offen offset:3072\n"
    "v_cvt_pk_bf16_f32 v80, v0, v1\n"
    "v_mfma_f32_16x16x32_bf16 v[8:11], a[12:15], v[44:47], v[8:11]\n"
    "v_exp_f32 v3, v3\n"
    "v_mfma_f32_16x16x32_bf16 v[12:15], a[16:19], v[32:35], -4.0\n"
    "v_cvt_pk_bf16_f32 v81, v2, v3\n"
    "v_exp_f32 v4, v4\n"
    "v_mfma_f32_16x16x32_bf16 v[12:15], a[20:23], v[36:39], v[12:15]\n"
    "v_exp_f32 v5, v5\n"
    "v_mfma_f32_16x16x32_bf16 v[12:15], a[24:27], v[40:43], v[12:15]\n"
    "v_exp_f32 v6, v6\n"
    "v_cvt_pk_bf16_f32 v82, v4, v5\n"
    "v_mfma_f32_16x16x32_bf16 v[12:15], a[28:31], v[44:47], v[12:15]\n"
    "v_exp_f32 v7, v7\n"
    "v_exp_f32 v8, v8\n"
    "s_xor_b32 s69, s72, 20480\n"
    "buffer_load_dwordx4 v[160:163], v231, s[44:47], s69 offen offset:0\n"
    "v_cvt_pk_bf16_f32 v83, v6, v7\n"
    "v_exp_f32 v9, v9\n"
    "ds_write_b128 v222, v[80:83] offset:8192\n"
    "v_exp_f32 v10, v10\n"
    "v_cvt_pk_bf16_f32 v96, v8, v9\n"
    "v_exp_f32 v11, v11\n"
    "v_exp_f32 v12, v12\n"
    "v_cvt_pk_bf16_f32 v97, v10, v11\n"
    "v_exp_f32 v13, v13\n"
    "v_exp_f32 v14, v14\n"
    "v_cvt_pk_bf16_f32 v98, v12, v13\n"
    "v_exp_f32 v15, v15\n"
    "s_nop 0\n"
    "v_cvt_pk_bf16_f32 v99, v14, v15\n"
    "ds_write_b128 v222, v[96:99] offset:12288\n"
    "s_waitcnt vmcnt(5)\n"
    "s_getpc_b64 s[78:79]\n"
    ".Lgqa_pc%=:\n"
    "s_lshl_b32 s68, s67, 12\n"
    "v_lshl_add_u32 v218, v233, 6, s68\n"
    "v_min_u32 v218, .Lgqa_slow%=-.Lgqa_pc%=, v218\n"
    "global_load_dword v242, v218, s[78:79]\n"
    "v_lshlrev_b32 v176, 16, v112\n"
    "v_and_b32 v177, s62, v112\n"
    "v_lshlrev_b32 v178, 16, v113\n"
    "v_and_b32 v179, s62, v113\n"
    "v_lshlrev_b32 v180, 16, v114\n"
    "v_and_b32 v181, s62, v114\n"
    "v_lshlrev_b32 v182, 16, v115\n"
    "v_and_b32 v183, s62, v115\n"
    "v_pk_mul_f32 v[176:177], v[176:177], s[60:61]\n"
    "v_pk_mul_f32 v[178:179], v[178:179], s[60:61]\n"
    "v_pk_mul_f32 v[180:181], v[180:181], s[60:61]\n"
    "v_pk_mul_f32 v[182:183], v[182:183], s[60:61]\n"
    "v_cvt_pk_bf16_f32 v112, v176, v177\n"
    "v_cvt_pk_bf16_f32 v113, v178, v179\n"
    "v_cvt_pk_bf16_f32 v114, v180, v181\n"
    "v_cvt_pk_bf16_f32 v115, v182, v183\n"
    "ds_write_b128 v240, v[112:115] offset:0\n"
    "v_lshlrev_b32 v184, 16, v116\n"
    "v_and_b32 v185, s62, v116\n"
    "v_lshlrev_b32 v186, 16, v117\n"
    "v_and_b32 v187, s62, v117\n"
    "v_lshlrev_b32 v188, 16, v118\n"
    "v_and_b32 v189, s62, v118\n"
    "v_lshlrev_b32 v190, 16, v119\n"
    "v_and_b32 v191, s62, v119\n"
    "v_pk_mul_f32 v[184:185], v[184:185], s[60:61]\n"
    "v_pk_mul_f32 v[186:187], v[186:187], s[60:61]\n"
    "v_pk_mul_f32 v[188:189], v[188:189], s[60:61]\n"
    "v_pk_mul_f32 v[190:191], v[190:191], s[60:61]\n"
    "v_cvt_pk_bf16_f32 v116, v184, v185\n"
    "v_cvt_pk_bf16_f32 v117, v186, v187\n"
    "v_cvt_pk_bf16_f32 v118, v188, v189\n"
    "v_cvt_pk_bf16_f32 v119, v190, v191\n"
    "ds_write_b128 v240, v[116:119] offset:1024\n"
    "v_lshlrev_b32 v192, 16, v120\n"
    "v_and_b32 v193, s62, v120\n"
    "v_lshlrev_b32 v194, 16, v121\n"
    "v_and_b32 v195, s62, v121\n"
    "buffer_load_dwordx4 v[164:167], v231, s[44:47], s69 offen offset:1024\n"
    "v_lshlrev_b32 v196, 16, v122\n"
    "v_and_b32 v197, s62, v122\n"
    "v_lshlrev_b32 v198, 16, v123\n"
    "v_and_b32 v199, s62, v123\n"
    "v_pk_mul_f32 v[192:193], v[192:193], s[60:61]\n"
    "v_pk_mul_f32 v[194:195], v[194:195], s[60:61]\n"
    "v_pk_mul_f32 v[196:197], v[196:197], s[60:61]\n"
    "v_pk_mul_f32 v[198:199], v[198:199], s[60:61]\n"
    "v_cvt_pk_bf16_f32 v120, v192, v193\n"
    "v_cvt_pk_bf16_f32 v121, v194, v195\n"
    "v_cvt_pk_bf16_f32 v122, v196, v197\n"
    "v_cvt_pk_bf16_f32 v123, v198, v199\n"
    "ds_write_b128 v241, v[120:123] offset:2048\n"
    "v_lshlrev_b32 v176, 16, v124\n"
    "v_and_b32 v177, s62, v124\n"
    "v_lshlrev_b32 v178, 16, v125\n"
    "v_and_b32 v179, s62, v125\n"
    "v_lshlrev_b32 v180, 16, v126\n"
    "v_and_b32 v181, s62, v126\n"
    "v_lshlrev_b32 v182, 16, v127\n"
    "v_and_b32 v183, s62, v127\n"
    "v_pk_mul_f32 v[176:177], v[176:177], s[60:61]\n"
    "v_pk_mul_f32 v[178:179], v[178:179], s[60:61]\n"
    "v_pk_mul_f32 v[180:181], v[180:181], s[60:61]\n"
    "v_pk_mul_f32 v[182:183], v[182:183], s[60:61]\n"
    "v_cvt_pk_bf16_f32 v124, v176, v177\n"
    "v_cvt_pk_bf16_f32 v125, v178, v179\n"
    "v_cvt_pk_bf16_f32 v126, v180, v181\n"
    "v_cvt_pk_bf16_f32 v127, v182, v183\n"
    "ds_write_b128 v241, v[124:127] offset:3072\n"
    "ds_read_b128 a[32:35], v232 offset:0\n"
    "ds_read_b128 a[36:39], v232 offset:256\n"
    "ds_read_b128 a[40:43], v232 offset:512\n"
    "ds_read_b128 a[44:47], v232 offset:768\n"
    "v_lshlrev_b32 v184, 16, v128\n"
    "v_and_b32 v185, s62, v128\n"
    "v_lshlrev_b32 v186, 16, v129\n"
    "v_and_b32 v187, s62, v129\n"
    "buffer_load_dwordx4 v[168:171], v231, s[44:47], s69 offen offset:2048\n"
    "v_lshlrev_b32 v188, 16, v130\n"
    "v_and_b32 v189, s62, v130\n"
    "v_lshlrev_b32 v190, 16, v131\n"
    "v_and_b32 v191, s62, v131\n"
    "v_pk_mul_f32 v[184:185], v[184:185], s[60:61]\n"
    "v_pk_mul_f32 v[186:187], v[186:187], s[60:61]\n"
    "v_pk_mul_f32 v[188:189], v[188:189], s[60:61]\n"
    "v_pk_mul_f32 v[190:191], v[190:191], s[60:61]\n"
    "v_cvt_pk_bf16_f32 v128, v184, v185\n"
    "v_cvt_pk_bf16_f32 v129, v186, v187\n"
    "v_cvt_pk_bf16_f32 v130, v188, v189\n"
    "v_cvt_pk_bf16_f32 v131, v190, v191\n"
    "ds_write_b128 v240, v[128:131] offset:0\n"
    "v_lshlrev_b32 v192, 16, v132\n"
    "v_and_b32 v193, s62, v132\n"
    "v_lshlrev_b32 v194, 16, v133\n"
    "v_and_b32 v195, s62, v133\n"
    "v_lshlrev_b32 v196, 16, v134\n"
    "v_and_b32 v197, s62, v134\n"
    "v_lshlrev_b32 v198, 16, v135\n"
    "v_and_b32 v199, s62, v135\n"
    "v_pk_mul_f32 v[192:193], v[192:193], s[60:61]\n"
    "v_pk_mul_f32 v[194:195], v[194:195], s[60:61]\n"
    "v_pk_mul_f32 v[196:197], v[196:197], s[60:61]\n"
    "v_pk_mul_f32 v[198:199], v[198:199], s[60:61]\n"
    "s_waitcnt lgkmcnt(1)\n"
    "v_mfma_f32_16x16x32_bf16 v[0:3], a[32:35], v[16:19], -4.0\n"
    "v_cvt_pk_bf16_f32 v132, v192, v193\n"
    "v_cvt_pk_bf16_f32 v133, v194, v195\n"
    "v_cvt_pk_bf16_f32 v134, v196, v197\n"
    "v_cvt_pk_bf16_f32 v135, v198, v199\n"
    "v_mfma_f32_16x16x32_bf16 v[0:3], a[36:39], v[20:23], v[0:3]\n"
    "ds_write_b128 v240, v[132:135] offset:1024\n"
    "v_lshlrev_b32 v176, 16, v136\n"
    "v_and_b32 v177, s62, v136\n"
    "v_mfma_f32_16x16x32_bf16 v[0:3], a[40:43], v[24:27], v[0:3]\n"
    "v_lshlrev_b32 v178, 16, v137\n"
    "v_and_b32 v179, s62, v137\n"
    "v_lshlrev_b32 v180, 16, v138\n"
    "buffer_load_dwordx4 v[172:175], v231, s[44:47], s69 offen offset:3072\n"
    "v_and_b32 v181, s62, v138\n"
    "v_lshlrev_b32 v182, 16, v139\n"
    "v_and_b32 v183, s62, v139\n"
    "v_pk_mul_f32 v[176:177], v[176:177], s[60:61]\n"
    "v_pk_mul_f32 v[178:179], v[178:179], s[60:61]\n"
    "v_pk_mul_f32 v[180:181], v[180:181], s[60:61]\n"
    "v_pk_mul_f32 v[182:183], v[182:183], s[60:61]\n"
    "v_mfma_f32_16x16x32_bf16 v[0:3], a[44:47], v[28:31], v[0:3]\n"
    "v_cvt_pk_bf16_f32 v136, v176, v177\n"
    "v_cvt_pk_bf16_f32 v137, v178, v179\n"
    "v_cvt_pk_bf16_f32 v138, v180, v181\n"
    "v_cvt_pk_bf16_f32 v139, v182, v183\n"
    "v_mfma_f32_16x16x32_bf16 v[4:7], a[32:35], v[32:35], -4.0\n"
    "ds_write_b128 v241, v[136:139] offset:2048\n"
    "v_lshlrev_b32 v184, 16, v140\n"
    "v_and_b32 v185, s62, v140\n"
    "v_mfma_f32_16x16x32_bf16 v[4:7], a[36:39], v[36:39], v[4:7]\n"
    "v_lshlrev_b32 v186, 16, v141\n"
    "v_and_b32 v187, s62, v141\n"
    "v_lshlrev_b32 v188, 16, v142\n"
    "v_and_b32 v189, s62, v142\n"
    "v_lshlrev_b32 v190, 16, v143\n"
    "v_and_b32 v191, s62, v143\n"
    "v_pk_mul_f32 v[184:185], v[184:185], s[60:61]\n"
    "v_pk_mul_f32 v[186:187], v[186:187], s[60:61]\n"
    "v_pk_mul_f32 v[188:189], v[188:189], s[60:61]\n"
    "v_pk_mul_f32 v[190:191], v[190:191], s[60:61]\n"
    "v_mfma_f32_16x16x32_bf16 v[4:7], a[40:43], v[40:43], v[4:7]\n"
    "v_cvt_pk_bf16_f32 v140, v184, v185\n"
    "v_cvt_pk_bf16_f32 v141, v186, v187\n"
    "v_cvt_pk_bf16_f32 v142, v188, v189\n"
    "v_cvt_pk_bf16_f32 v143, v190, v191\n"
    "v_mfma_f32_16x16x32_bf16 v[4:7], a[44:47], v[44:47], v[4:7]\n"
    "ds_write_b128 v241, v[140:143] offset:3072\n"
    "ds_read_b128 a[48:51], v232 offset:0\n"
    "ds_read_b128 a[52:55], v232 offset:256\n"
    "ds_read_b128 a[56:59], v232 offset:512\n"
    "ds_read_b128 a[60:63], v232 offset:768\n"
    "s_xor_b32 s69, s72, 24576\n"
    "buffer_load_dwordx4 v[112:115], v231, s[44:47], s69 offen offset:0\n"
    "v_exp_f32 v0, v0\n"
    "v_exp_f32 v1, v1\n"
    "v_exp_f32 v2, v2\n"
    "v_cvt_pk_bf16_f32 v84, v0, v1\n"
    "v_exp_f32 v3, v3\n"
    "s_waitcnt lgkmcnt(3)\n"
    "v_mfma_f32_16x16x32_bf16 v[8:11], a[48:51], v[16:19], -4.0\n"
    "v_cvt_pk_bf16_f32 v85, v2, v3\n"
    "v_exp_f32 v4, v4\n"
    "s_waitcnt lgkmcnt(2)\n"
    "v_mfma_f32_16x16x32_bf16 v[8:11], a[52:55], v[20:23], v[8:11]\n"
    "v_exp_f32 v5, v5\n"
    "buffer_load_dwordx4 v[116:119], v231, s[44:47], s69 offen offset:1024\n"
    "s_waitcnt lgkmcnt(0)\n"
    "v_mfma_f32_16x16x32_bf16 v[8:11], a[56:59], v[24:27], v[8:11]\n"
    "v_exp_f32 v6, v6\n"
    "v_cvt_pk_bf16_f32 v100, v4, v5\n"
    "v_mfma_f32_16x16x32_bf16 v[8:11], a[60:63], v[28:31], v[8:11]\n"
    "v_exp_f32 v7, v7\n"
    "v_mfma_f32_16x16x32_bf16 v[12:15], a[48:51], v[32:35], -4.0\n"
    "ds_read_b128 v[16:19], v221 offset:0\n"
    "v_cvt_pk_bf16_f32 v101, v6, v7\n"
    "v_mfma_f32_16x16x32_bf16 v[12:15], a[52:55], v[36:39], v[12:15]\n"
    "ds_read_b128 v[20:23], v221 offset:256\n"
    "v_mfma_f32_16x16x32_bf16 v[12:15], a[56:59], v[40:43], v[12:15]\n"
    "buffer_load_dwordx4 v[120:123], v231, s[44:47], s69 offen offset:2048\n"
    "ds_read_b128 v[24:27], v221 offset:512\n"
    "v_mfma_f32_16x16x32_bf16 v[12:15], a[60:63], v[44:47], v[12:15]\n"
    "ds_read_b128 v[28:31], v221 offset:768\n"
    "ds_read_b128 v[32:35], v221 offset:32768\n"
    "ds_read_b128 v[36:39], v221 offset:33024\n"
    "ds_read_b128 v[40:43], v221 offset:33280\n"
    "ds_read_b128 v[44:47], v221 offset:33536\n"
    "v_exp_f32 v8, v8\n"
    "v_exp_f32 v9, v9\n"
    "v_exp_f32 v10, v10\n"
    "buffer_load_dwordx4 v[124:127], v231, s[44:47], s69 offen offset:3072\n"
    "v_cvt_pk_bf16_f32 v86, v8, v9\n"
    "v_exp_f32 v11, v11\n"
    "s_waitcnt lgkmcnt(6)\n"
    "v_mfma_f32_16x16x32_bf16 v[0:3], a[32:35], v[16:19], -4.0\n"
    "v_cvt_pk_bf16_f32 v87, v10, v11\n"
    "v_exp_f32 v12, v12\n"
    "v_mfma_f32_16x16x32_bf16 v[0:3], a[36:39], v[20:23], v[0:3]\n"
    "ds_write_b128 v222, v[84:87] offset:9216\n"
    "v_exp_f32 v13, v13\n"
    "s_waitcnt lgkmcnt(1)\n"
    "v_mfma_f32_16x16x32_bf16 v[0:3], a[40:43], v[24:27], v[0:3]\n"
    "v_exp_f32 v14, v14\n"
    "v_cvt_pk_bf16_f32 v102, v12, v13\n"
    "s_xor_b32 s69, s72, 28672\n"
    "buffer_load_dwordx4 v[128:131], v231, s[44:47], s69 offen offset:0\n"
    "v_mfma_f32_16x16x32_bf16 v[0:3], a[44:47], v[28:31], v[0:3]\n"
    "v_exp_f32 v15, v15\n"
    "v_mfma_f32_16x16x32_bf16 v[4:7], a[48:51], v[16:19], -4.0\n"
    "v_cvt_pk_bf16_f32 v103, v14, v15\n"
    "v_mfma_f32_16x16x32_bf16 v[4:7], a[52:55], v[20:23], v[4:7]\n"
    "ds_write_b128 v222, v[100:103] offset:13312\n"
    "v_mfma_f32_16x16x32_bf16 v[4:7], a[56:59], v[24:27], v[4:7]\n"
    "v_mfma_f32_16x16x32_bf16 v[4:7], a[60:63], v[28:31], v[4:7]\n"
    "v_mfma_f32_16x16x32_bf16 v[8:11], a[32:35], v[32:35], -4.0\n"
    "v_exp_f32 v0, v0\n"
    "buffer_load_dwordx4 v[132:135], v231, s[44:47], s69 offen offset:1024\n"
    "v_mfma_f32_16x16x32_bf16 v[8:11], a[36:39], v[36:39], v[8:11]\n"
    "v_exp_f32 v1, v1\n"
    "v_mfma_f32_16x16x32_bf16 v[8:11], a[40:43], v[40:43], v[8:11]\n"
    "v_exp_f32 v2, v2\n"
    "v_cvt_pk_bf16_f32 v52, v0, v1\n"
    "v_mfma_f32_16x16x32_bf16 v[8:11], a[44:47], v[44:47], v[8:11]\n"
    "v_exp_f32 v3, v3\n"
    "v_mfma_f32_16x16x32_bf16 v[12:15], a[48:51], v[32:35], -4.0\n"
    "v_cvt_pk_bf16_f32 v53, v2, v3\n"
    "v_exp_f32 v4, v4\n"
    "v_mfma_f32_16x16x32_bf16 v[12:15], a[52:55], v[36:39], v[12:15]\n"
    "buffer_load_dwordx4 v[136:139], v231, s[44:47], s69 offen offset:2048\n"
    "v_exp_f32 v5, v5\n"
    "v_mfma_f32_16x16x32_bf16 v[12:15], a[56:59], v[40:43], v[12:15]\n"
    "v_exp_f32 v6, v6\n"
    "v_cvt_pk_bf16_f32 v54, v4, v5\n"
    "v_mfma_f32_16x16x32_bf16 v[12:15], a[60:63], v[44:47], v[12:15]\n"
    "v_exp_f32 v7, v7\n"
    "v_exp_f32 v8, v8\n"
    "v_cvt_pk_bf16_f32 v55, v6, v7\n"
    "v_exp_f32 v9, v9\n"
    "ds_write_b128 v222, v[52:55] offset:1024\n"
    "buffer_load_dwordx4 v[140:143], v231, s[44:47], s69 offen offset:3072\n"
    "v_exp_f32 v10, v10\n"
    "v_cvt_pk_bf16_f32 v68, v8, v9\n"
    "v_exp_f32 v11, v11\n"
    "v_exp_f32 v12, v12\n"
    "v_cvt_pk_bf16_f32 v69, v10, v11\n"
    "v_exp_f32 v13, v13\n"
    "v_exp_f32 v14, v14\n"
    "v_cvt_pk_bf16_f32 v70, v12, v13\n"
    "v_exp_f32 v15, v15\n"
    "s_nop 0\n"
    "v_cvt_pk_bf16_f32 v71, v14, v15\n"
    "ds_write_b128 v222, v[68:71] offset:5120\n"
    "s_waitcnt vmcnt(8)\n"
    "s_xor_b32 m0, s66, 0\n"
    "s_xor_b32 s70, s72, 0\n"
    "buffer_load_dwordx4 v231, s[48:51], s70 offen offset:0 lds\n"
    "v_lshlrev_b32 v176, 16, v144\n"
    "v_and_b32 v177, s62, v144\n"
    "v_lshlrev_b32 v178, 16, v145\n"
    "v_and_b32 v179, s62, v145\n"
    "v_lshlrev_b32 v180, 16, v146\n"
    "v_and_b32 v181, s62, v146\n"
    "v_lshlrev_b32 v182, 16, v147\n"
    "v_and_b32 v183, s62, v147\n"
    "v_pk_mul_f32 v[176:177], v[176:177], s[60:61]\n"
    "v_pk_mul_f32 v[178:179], v[178:179], s[60:61]\n"
    "v_pk_mul_f32 v[180:181], v[180:181], s[60:61]\n"
    "v_pk_mul_f32 v[182:183], v[182:183], s[60:61]\n"
    "v_cvt_pk_bf16_f32 v144, v176, v177\n"
    "v_cvt_pk_bf16_f32 v145, v178, v179\n"
    "v_cvt_pk_bf16_f32 v146, v180, v181\n"
    "v_cvt_pk_bf16_f32 v147, v182, v183\n"
    "ds_write_b128 v240, v[144:147] offset:0\n"
    "v_lshlrev_b32 v184, 16, v148\n"
    "v_and_b32 v185, s62, v148\n"
    "v_lshlrev_b32 v186, 16, v149\n"
    "v_and_b32 v187, s62, v149\n"
    "v_lshlrev_b32 v188, 16, v150\n"
    "v_and_b32 v189, s62, v150\n"
    "v_lshlrev_b32 v190, 16, v151\n"
    "v_and_b32 v191, s62, v151\n"
    "v_pk_mul_f32 v[184:185], v[184:185], s[60:61]\n"
    "v_pk_mul_f32 v[186:187], v[186:187], s[60:61]\n"
    "v_pk_mul_f32 v[188:189], v[188:189], s[60:61]\n"
    "v_pk_mul_f32 v[190:191], v[190:191], s[60:61]\n"
    "v_cvt_pk_bf16_f32 v148, v184, v185\n"
    "v_cvt_pk_bf16_f32 v149, v186, v187\n"
    "v_cvt_pk_bf16_f32 v150, v188, v189\n"
    "v_cvt_pk_bf16_f32 v151, v190, v191\n"
    "ds_write_b128 v240, v[148:151] offset:1024\n"
    "v_lshlrev_b32 v192, 16, v152\n"
    "v_and_b32 v193, s62, v152\n"
    "v_lshlrev_b32 v194, 16, v153\n"
    "v_and_b32 v195, s62, v153\n"
    "buffer_load_dwordx4 v231, s[48:51], s70 offen offset:1024 lds\n"
    "v_lshlrev_b32 v196, 16, v154\n"
    "v_and_b32 v197, s62, v154\n"
    "v_lshlrev_b32 v198, 16, v155\n"
    "v_and_b32 v199, s62, v155\n"
    "v_pk_mul_f32 v[192:193], v[192:193], s[60:61]\n"
    "v_pk_mul_f32 v[194:195], v[194:195], s[60:61]\n"
    "v_pk_mul_f32 v[196:197], v[196:197], s[60:61]\n"
    "v_pk_mul_f32 v[198:199], v[198:199], s[60:61]\n"
    "v_cvt_pk_bf16_f32 v152, v192, v193\n"
    "v_cvt_pk_bf16_f32 v153, v194, v195\n"
    "v_cvt_pk_bf16_f32 v154, v196, v197\n"
    "v_cvt_pk_bf16_f32 v155, v198, v199\n"
    "ds_write_b128 v241, v[152:155] offset:2048\n"
    "v_lshlrev_b32 v176, 16, v156\n"
    "v_and_b32 v177, s62, v156\n"
    "v_lshlrev_b32 v178, 16, v157\n"
    "v_and_b32 v179, s62, v157\n"
    "v_lshlrev_b32 v180, 16, v158\n"
    "v_and_b32 v181, s62, v158\n"
    "v_lshlrev_b32 v182, 16, v159\n"
    "v_and_b32 v183, s62, v159\n"
    "v_pk_mul_f32 v[176:177], v[176:177], s[60:61]\n"
    "v_pk_mul_f32 v[178:179], v[178:179], s[60:61]\n"
    "v_pk_mul_f32 v[180:181], v[180:181], s[60:61]\n"
    "v_pk_mul_f32 v[182:183], v[182:183], s[60:61]\n"
    "v_cvt_pk_bf16_f32 v156, v176, v177\n"
    "v_cvt_pk_bf16_f32 v157, v178, v179\n"
    "v_cvt_pk_bf16_f32 v158, v180, v181\n"
    "v_cvt_pk_bf16_f32 v159, v182, v183\n"
    "ds_write_b128 v241, v[156:159] offset:3072\n"
    "ds_read_b128 a[64:67], v232 offset:0\n"
    "ds_read_b128 a[68:71], v232 offset:256\n"
    "ds_read_b128 a[72:75], v232 offset:512\n"
    "ds_read_b128 a[76:79], v232 offset:768\n"
    "v_lshlrev_b32 v184, 16, v160\n"
    "v_and_b32 v185, s62, v160\n"
    "v_lshlrev_b32 v186, 16, v161\n"
    "v_and_b32 v187, s62, v161\n"
    "buffer_load_dwordx4 v231, s[48:51], s70 offen offset:2048 lds\n"
    "v_lshlrev_b32 v188, 16, v162\n"
    "v_and_b32 v189, s62, v162\n"
    "v_lshlrev_b32 v190, 16, v163\n"
    "v_and_b32 v191, s62, v163\n"
    "v_pk_mul_f32 v[184:185], v[184:185], s[60:61]\n"
    "v_pk_mul_f32 v[186:187], v[186:187], s[60:61]\n"
    "v_pk_mul_f32 v[188:189], v[188:189], s[60:61]\n"
    "v_pk_mul_f32 v[190:191], v[190:191], s[60:61]\n"
    "v_cvt_pk_bf16_f32 v160, v184, v185\n"
    "v_cvt_pk_bf16_f32 v161, v186, v187\n"
    "v_cvt_pk_bf16_f32 v162, v188, v189\n"
    "v_cvt_pk_bf16_f32 v163, v190, v191\n"
    "ds_write_b128 v240, v[160:163] offset:0\n"
    "v_lshlrev_b32 v192, 16, v164\n"
    "v_and_b32 v193, s62, v164\n"
    "v_lshlrev_b32 v194, 16, v165\n"
    "v_and_b32 v195, s62, v165\n"
    "v_lshlrev_b32 v196, 16, v166\n"
    "v_and_b32 v197, s62, v166\n"
    "v_lshlrev_b32 v198, 16, v167\n"
    "v_and_b32 v199, s62, v167\n"
    "v_pk_mul_f32 v[192:193], v[192:193], s[60:61]\n"
    "v_pk_mul_f32 v[194:195], v[194:195], s[60:61]\n"
    "v_pk_mul_f32 v[196:197], v[196:197], s[60:61]\n"
    "v_pk_mul_f32 v[198:199], v[198:199], s[60:61]\n"
    "s_waitcnt lgkmcnt(1)\n"
    "v_mfma_f32_16x16x32_bf16 v[0:3], a[64:67], v[16:19], -4.0\n"
    "v_cvt_pk_bf16_f32 v164, v192, v193\n"
    "v_cvt_pk_bf16_f32 v165, v194, v195\n"
    "v_cvt_pk_bf16_f32 v166, v196, v197\n"
    "v_cvt_pk_bf16_f32 v167, v198, v199\n"
    "v_mfma_f32_16x16x32_bf16 v[0:3], a[68:71], v[20:23], v[0:3]\n"
    "ds_write_b128 v240, v[164:167] offset:1024\n"
    "v_lshlrev_b32 v176, 16, v168\n"
    "v_and_b32 v177, s62, v168\n"
    "v_mfma_f32_16x16x32_bf16 v[0:3], a[72:75], v[24:27], v[0:3]\n"
    "v_lshlrev_b32 v178, 16, v169\n"
    "v_and_b32 v179, s62, v169\n"
    "v_lshlrev_b32 v180, 16, v170\n"
    "buffer_load_dwordx4 v231, s[48:51], s70 offen offset:3072 lds\n"
    "v_and_b32 v181, s62, v170\n"
    "v_lshlrev_b32 v182, 16, v171\n"
    "v_and_b32 v183, s62, v171\n"
    "v_pk_mul_f32 v[176:177], v[176:177], s[60:61]\n"
    "v_pk_mul_f32 v[178:179], v[178:179], s[60:61]\n"
    "v_pk_mul_f32 v[180:181], v[180:181], s[60:61]\n"
    "v_pk_mul_f32 v[182:183], v[182:183], s[60:61]\n"
    "v_mfma_f32_16x16x32_bf16 v[0:3], a[76:79], v[28:31], v[0:3]\n"
    "v_cvt_pk_bf16_f32 v168, v176, v177\n"
    "v_cvt_pk_bf16_f32 v169, v178, v179\n"
    "v_cvt_pk_bf16_f32 v170, v180, v181\n"
    "v_cvt_pk_bf16_f32 v171, v182, v183\n"
    "v_mfma_f32_16x16x32_bf16 v[4:7], a[64:67], v[32:35], -4.0\n"
    "ds_write_b128 v241, v[168:171] offset:2048\n"
    "v_lshlrev_b32 v184, 16, v172\n"
    "v_and_b32 v185, s62, v172\n"
    "v_mfma_f32_16x16x32_bf16 v[4:7], a[68:71], v[36:39], v[4:7]\n"
    "v_lshlrev_b32 v186, 16, v173\n"
    "v_and_b32 v187, s62, v173\n"
    "v_lshlrev_b32 v188, 16, v174\n"
    "v_and_b32 v189, s62, v174\n"
    "v_lshlrev_b32 v190, 16, v175\n"
    "v_and_b32 v191, s62, v175\n"
    "v_pk_mul_f32 v[184:185], v[184:185], s[60:61]\n"
    "v_pk_mul_f32 v[186:187], v[186:187], s[60:61]\n"
    "v_pk_mul_f32 v[188:189], v[188:189], s[60:61]\n"
    "v_pk_mul_f32 v[190:191], v[190:191], s[60:61]\n"
    "v_mfma_f32_16x16x32_bf16 v[4:7], a[72:75], v[40:43], v[4:7]\n"
    "v_cvt_pk_bf16_f32 v172, v184, v185\n"
    "v_cvt_pk_bf16_f32 v173, v186, v187\n"
    "v_cvt_pk_bf16_f32 v174, v188, v189\n"
    "v_cvt_pk_bf16_f32 v175, v190, v191\n"
    "v_mfma_f32_16x16x32_bf16 v[4:7], a[76:79], v[44:47], v[4:7]\n"
    "ds_write_b128 v241, v[172:175] offset:3072\n"
    "ds_read_b128 a[80:83], v232 offset:0\n"
    "ds_read_b128 a[84:87], v232 offset:256\n"
    "ds_read_b128 a[88:91], v232 offset:512\n"
    "ds_read_b128 a[92:95], v232 offset:768\n"
    "s_xor_b32 m0, s66, 4096\n"
    "s_xor_b32 s70, s72, 4096\n"
    "buffer_load_dwordx4 v231, s[48:51], s70 offen offset:0 lds\n"
    "v_exp_f32 v0, v0\n"
    "v_exp_f32 v1, v1\n"
    "v_exp_f32 v2, v2\n"
    "v_cvt_pk_bf16_f32 v56, v0, v1\n"
    "v_exp_f32 v3, v3\n"
    "s_waitcnt lgkmcnt(3)\n"
    "v_mfma_f32_16x16x32_bf16 v[8:11], a[80:83], v[16:19], -4.0\n"
    "v_cvt_pk_bf16_f32 v57, v2, v3\n"
    "v_exp_f32 v4, v4\n"
    "s_waitcnt lgkmcnt(2)\n"
    "v_mfma_f32_16x16x32_bf16 v[8:11], a[84:87], v[20:23], v[8:11]\n"
    "v_exp_f32 v5, v5\n"
    "s_waitcnt lgkmcnt(0)\n"
    "v_mfma_f32_16x16x32_bf16 v[8:11], a[88:91], v[24:27], v[8:11]\n"
    "v_exp_f32 v6, v6\n"
    "v_cvt_pk_bf16_f32 v72, v4, v5\n"
    "v_mfma_f32_16x16x32_bf16 v[8:11], a[92:95], v[28:31], v[8:11]\n"
    "v_exp_f32 v7, v7\n"
    "v_mfma_f32_16x16x32_bf16 v[12:15], a[80:83], v[32:35], -4.0\n"
    "ds_read_b128 v[16:19], v221 offset:2048\n"
    "v_cvt_pk_bf16_f32 v73, v6, v7\n"
    "v_mfma_f32_16x16x32_bf16 v[12:15], a[84:87], v[36:39], v[12:15]\n"
    "ds_read_b128 v[20:23], v221 offset:2304\n"
    "v_mfma_f32_16x16x32_bf16 v[12:15], a[88:91], v[40:43], v[12:15]\n"
    "buffer_load_dwordx4 v231, s[48:51], s70 offen offset:1024 lds\n"
    "ds_read_b128 v[24:27], v221 offset:2560\n"
    "v_mfma_f32_16x16x32_bf16 v[12:15], a[92:95], v[44:47], v[12:15]\n"
    "ds_read_b128 v[28:31], v221 offset:2816\n"
    "ds_read_b128 v[32:35], v221 offset:34816\n"
    "ds_read_b128 v[36:39], v221 offset:35072\n"
    "ds_read_b128 v[40:43], v221 offset:35328\n"
    "ds_read_b128 v[44:47], v221 offset:35584\n"
    "v_exp_f32 v8, v8\n"
    "v_exp_f32 v9, v9\n"
    "v_exp_f32 v10, v10\n"
    "v_cvt_pk_bf16_f32 v58, v8, v9\n"
    "v_exp_f32 v11, v11\n"
    "s_waitcnt lgkmcnt(7)\n"
    "v_mfma_f32_16x16x32_bf16 v[0:3], a[64:67], v[16:19], -4.0\n"
    "v_cvt_pk_bf16_f32 v59, v10, v11\n"
    "v_exp_f32 v12, v12\n"
    "s_waitcnt lgkmcnt(6)\n"
    "v_mfma_f32_16x16x32_bf16 v[0:3], a[68:71], v[20:23], v[0:3]\n"
    "ds_write_b128 v222, v[56:59] offset:2048\n"
    "v_exp_f32 v13, v13\n"
    "s_waitcnt lgkmcnt(3)\n"
    "v_mfma_f32_16x16x32_bf16 v[0:3], a[72:75], v[24:27], v[0:3]\n"
    "v_exp_f32 v14, v14\n"
    "v_cvt_pk_bf16_f32 v74, v12, v13\n"
    "buffer_load_dwordx4 v231, s[48:51], s70 offen offset:2048 lds\n"
    "v_mfma_f32_16x16x32_bf16 v[0:3], a[76:79], v[28:31], v[0:3]\n"
    "v_exp_f32 v15, v15\n"
    "v_mfma_f32_16x16x32_bf16 v[4:7], a[80:83], v[16:19], -4.0\n"
    "v_cvt_pk_bf16_f32 v75, v14, v15\n"
    "v_mfma_f32_16x16x32_bf16 v[4:7], a[84:87], v[20:23], v[4:7]\n"
    "ds_write_b128 v222, v[72:75] offset:6144\n"
    "v_mfma_f32_16x16x32_bf16 v[4:7], a[88:91], v[24:27], v[4:7]\n"
    "v_mfma_f32_16x16x32_bf16 v[4:7], a[92:95], v[28:31], v[4:7]\n"
    "v_mfma_f32_16x16x32_bf16 v[8:11], a[64:67], v[32:35], -4.0\n"
    "v_exp_f32 v0, v0\n"
    "v_mfma_f32_16x16x32_bf16 v[8:11], a[68:71], v[36:39], v[8:11]\n"
    "v_exp_f32 v1, v1\n"
    "s_waitcnt lgkmcnt(1)\n"
    "v_mfma_f32_16x16x32_bf16 v[8:11], a[72:75], v[40:43], v[8:11]\n"
    "v_exp_f32 v2, v2\n"
    "v_cvt_pk_bf16_f32 v88, v0, v1\n"
    "v_mfma_f32_16x16x32_bf16 v[8:11], a[76:79], v[44:47], v[8:11]\n"
    "v_exp_f32 v3, v3\n"
    "v_mfma_f32_16x16x32_bf16 v[12:15], a[80:83], v[32:35], -4.0\n"
    "v_cvt_pk_bf16_f32 v89, v2, v3\n"
    "v_exp_f32 v4, v4\n"
    "v_mfma_f32_16x16x32_bf16 v[12:15], a[84:87], v[36:39], v[12:15]\n"
    "buffer_load_dwordx4 v231, s[48:51], s70 offen offset:3072 lds\n"
    "v_exp_f32 v5, v5\n"
    "v_mfma_f32_16x16x32_bf16 v[12:15], a[88:91], v[40:43], v[12:15]\n"
    "v_exp_f32 v6, v6\n"
    "v_cvt_pk_bf16_f32 v90, v4, v5\n"
    "v_mfma_f32_16x16x32_bf16 v[12:15], a[92:95], v[44:47], v[12:15]\n"
    "v_exp_f32 v7, v7\n"
    "v_exp_f32 v8, v8\n"
    "v_cvt_pk_bf16_f32 v91, v6, v7\n"
    "v_exp_f32 v9, v9\n"
    "ds_write_b128 v222, v[88:91] offset:10240\n"
    "v_exp_f32 v10, v10\n"
    "v_cvt_pk_bf16_f32 v104, v8, v9\n"
    "v_exp_f32 v11, v11\n"
    "v_exp_f32 v12, v12\n"
    "v_cvt_pk_bf16_f32 v105, v10, v11\n"
    "v_exp_f32 v13, v13\n"
    "v_exp_f32 v14, v14\n"
    "v_cvt_pk_bf16_f32 v106, v12, v13\n"
    "v_exp_f32 v15, v15\n"
    "s_nop 0\n"
    "v_cvt_pk_bf16_f32 v107, v14, v15\n"
    "ds_write_b128 v222, v[104:107] offset:14336\n"
    "s_waitcnt vmcnt(8)\n"
    "s_add_u32 m0, s81, 0\n"
    "s_xor_b32 s70, s72, 8192\n"
    "buffer_load_dwordx4 v231, s[48:51], s70 offen offset:0 lds\n"
    "v_lshlrev_b32 v176, 16, v112\n"
    "v_and_b32 v177, s62, v112\n"
    "v_lshlrev_b32 v178, 16, v113\n"
    "v_and_b32 v179, s62, v113\n"
    "v_lshlrev_b32 v180, 16, v114\n"
    "v_and_b32 v181, s62, v114\n"
    "v_lshlrev_b32 v182, 16, v115\n"
    "v_and_b32 v183, s62, v115\n"
    "v_pk_mul_f32 v[176:177], v[176:177], s[60:61]\n"
    "v_pk_mul_f32 v[178:179], v[178:179], s[60:61]\n"
    "v_pk_mul_f32 v[180:181], v[180:181], s[60:61]\n"
    "v_pk_mul_f32 v[182:183], v[182:183], s[60:61]\n"
    "v_cvt_pk_bf16_f32 v112, v176, v177\n"
    "v_cvt_pk_bf16_f32 v113, v178, v179\n"
    "v_cvt_pk_bf16_f32 v114, v180, v181\n"
    "v_cvt_pk_bf16_f32 v115, v182, v183\n"
    "ds_write_b128 v240, v[112:115] offset:0\n"
    "v_lshlrev_b32 v184, 16, v116\n"
    "v_and_b32 v185, s62, v116\n"
    "v_lshlrev_b32 v186, 16, v117\n"
    "v_and_b32 v187, s62, v117\n"
    "v_lshlrev_b32 v188, 16, v118\n"
    "v_and_b32 v189, s62, v118\n"
    "v_lshlrev_b32 v190, 16, v119\n"
    "v_and_b32 v191, s62, v119\n"
    "v_pk_mul_f32 v[184:185], v[184:185], s[60:61]\n"
    "v_pk_mul_f32 v[186:187], v[186:187], s[60:61]\n"
    "v_pk_mul_f32 v[188:189], v[188:189], s[60:61]\n"
    "v_pk_mul_f32 v[190:191], v[190:191], s[60:61]\n"
    "v_cvt_pk_bf16_f32 v116, v184, v185\n"
    "buffer_load_dwordx4 v231, s[48:51], s70 offen offset:1024 lds\n"
    "v_cvt_pk_bf16_f32 v117, v186, v187\n"
    "v_cvt_pk_bf16_f32 v118, v188, v189\n"
    "v_cvt_pk_bf16_f32 v119, v190, v191\n"
    "ds_write_b128 v240, v[116:119] offset:1024\n"
    "v_lshlrev_b32 v192, 16, v120\n"
    "v_and_b32 v193, s62, v120\n"
    "v_lshlrev_b32 v194, 16, v121\n"
    "v_and_b32 v195, s62, v121\n"
    "v_lshlrev_b32 v196, 16, v122\n"
    "v_and_b32 v197, s62, v122\n"
    "v_lshlrev_b32 v198, 16, v123\n"
    "v_and_b32 v199, s62, v123\n"
    "v_pk_mul_f32 v[192:193], v[192:193], s[60:61]\n"
    "v_pk_mul_f32 v[194:195], v[194:195], s[60:61]\n"
    "v_pk_mul_f32 v[196:197], v[196:197], s[60:61]\n"
    "v_pk_mul_f32 v[198:199], v[198:199], s[60:61]\n"
    "v_cvt_pk_bf16_f32 v120, v192, v193\n"
    "v_cvt_pk_bf16_f32 v121, v194, v195\n"
    "v_cvt_pk_bf16_f32 v122, v196, v197\n"
    "v_cvt_pk_bf16_f32 v123, v198, v199\n"
    "ds_write_b128 v241, v[120:123] offset:2048\n"
    "v_lshlrev_b32 v176, 16, v124\n"
    "v_and_b32 v177, s62, v124\n"
    "v_lshlrev_b32 v178, 16, v125\n"
    "v_and_b32 v179, s62, v125\n"
    "v_lshlrev_b32 v180, 16, v126\n"
    "v_and_b32 v181, s62, v126\n"
    "v_lshlrev_b32 v182, 16, v127\n"
    "v_and_b32 v183, s62, v127\n"
    "v_pk_mul_f32 v[176:177], v[176:177], s[60:61]\n"
    "buffer_load_dwordx4 v231, s[48:51], s70 offen offset:2048 lds\n"
    "v_pk_mul_f32 v[178:179], v[178:179], s[60:61]\n"
    "v_pk_mul_f32 v[180:181], v[180:181], s[60:61]\n"
    "v_pk_mul_f32 v[182:183], v[182:183], s[60:61]\n"
    "v_cvt_pk_bf16_f32 v124, v176, v177\n"
    "v_cvt_pk_bf16_f32 v125, v178, v179\n"
    "v_cvt_pk_bf16_f32 v126, v180, v181\n"
    "v_cvt_pk_bf16_f32 v127, v182, v183\n"
    "ds_write_b128 v241, v[124:127] offset:3072\n"
    "ds_read_b128 a[96:99], v232 offset:0\n"
    "ds_read_b128 a[100:103], v232 offset:256\n"
    "ds_read_b128 a[104:107], v232 offset:512\n"
    "ds_read_b128 a[108:111], v232 offset:768\n"
    "v_lshlrev_b32 v184, 16, v128\n"
    "v_and_b32 v185, s62, v128\n"
    "v_lshlrev_b32 v186, 16, v129\n"
    "v_and_b32 v187, s62, v129\n"
    "v_lshlrev_b32 v188, 16, v130\n"
    "v_and_b32 v189, s62, v130\n"
    "v_lshlrev_b32 v190, 16, v131\n"
    "v_and_b32 v191, s62, v131\n"
    "v_pk_mul_f32 v[184:185], v[184:185], s[60:61]\n"
    "v_pk_mul_f32 v[186:187], v[186:187], s[60:61]\n"
    "v_pk_mul_f32 v[188:189], v[188:189], s[60:61]\n"
    "v_pk_mul_f32 v[190:191], v[190:191], s[60:61]\n"
    "v_cvt_pk_bf16_f32 v128, v184, v185\n"
    "v_cvt_pk_bf16_f32 v129, v186, v187\n"
    "v_cvt_pk_bf16_f32 v130, v188, v189\n"
    "v_cvt_pk_bf16_f32 v131, v190, v191\n"
    "ds_write_b128 v240, v[128:131] offset:0\n"
    "v_lshlrev_b32 v192, 16, v132\n"
    "v_and_b32 v193, s62, v132\n"
    "buffer_load_dwordx4 v231, s[48:51], s70 offen offset:3072 lds\n"
    "v_lshlrev_b32 v194, 16, v133\n"
    "v_and_b32 v195, s62, v133\n"
    "v_lshlrev_b32 v196, 16, v134\n"
    "v_and_b32 v197, s62, v134\n"
    "v_lshlrev_b32 v198, 16, v135\n"
    "v_and_b32 v199, s62, v135\n"
    "v_pk_mul_f32 v[192:193], v[192:193], s[60:61]\n"
    "v_pk_mul_f32 v[194:195], v[194:195], s[60:61]\n"
    "v_pk_mul_f32 v[196:197], v[196:197], s[60:61]\n"
    "v_pk_mul_f32 v[198:199], v[198:199], s[60:61]\n"
    "s_waitcnt lgkmcnt(1)\n"
    "v_mfma_f32_16x16x32_bf16 v[0:3], a[96:99], v[16:19], -4.0\n"
    "v_cvt_pk_bf16_f32 v132, v192, v193\n"
    "v_cvt_pk_bf16_f32 v133, v194, v195\n"
    "v_cvt_pk_bf16_f32 v134, v196, v197\n"
    "v_cvt_pk_bf16_f32 v135, v198, v199\n"
    "v_mfma_f32_16x16x32_bf16 v[0:3], a[100:103], v[20:23], v[0:3]\n"
    "ds_write_b128 v240, v[132:135] offset:1024\n"
    "v_lshlrev_b32 v176, 16, v136\n"
    "v_and_b32 v177, s62, v136\n"
    "v_mfma_f32_16x16x32_bf16 v[0:3], a[104:107], v[24:27], v[0:3]\n"
    "v_lshlrev_b32 v178, 16, v137\n"
    "v_and_b32 v179, s62, v137\n"
    "v_lshlrev_b32 v180, 16, v138\n"
    "v_and_b32 v181, s62, v138\n"
    "v_lshlrev_b32 v182, 16, v139\n"
    "v_and_b32 v183, s62, v139\n"
    "v_pk_mul_f32 v[176:177], v[176:177], s[60:61]\n"
    "v_pk_mul_f32 v[178:179], v[178:179], s[60:61]\n"
    "v_pk_mul_f32 v[180:181], v[180:181], s[60:61]\n"
    "v_pk_mul_f32 v[182:183], v[182:183], s[60:61]\n"
    "s_add_u32 m0, s81, 4096\n"
    "s_xor_b32 s70, s72, 12288\n"
    "buffer_load_dwordx4 v231, s[48:51], s70 offen offset:0 lds\n"
    "v_mfma_f32_16x16x32_bf16 v[0:3], a[108:111], v[28:31], v[0:3]\n"
    "v_cvt_pk_bf16_f32 v136, v176, v177\n"
    "v_cvt_pk_bf16_f32 v137, v178, v179\n"
    "v_cvt_pk_bf16_f32 v138, v180, v181\n"
    "v_cvt_pk_bf16_f32 v139, v182, v183\n"
    "v_mfma_f32_16x16x32_bf16 v[4:7], a[96:99], v[32:35], -4.0\n"
    "ds_write_b128 v241, v[136:139] offset:2048\n"
    "v_lshlrev_b32 v184, 16, v140\n"
    "v_and_b32 v185, s62, v140\n"
    "v_mfma_f32_16x16x32_bf16 v[4:7], a[100:103], v[36:39], v[4:7]\n"
    "v_lshlrev_b32 v186, 16, v141\n"
    "v_and_b32 v187, s62, v141\n"
    "v_lshlrev_b32 v188, 16, v142\n"
    "v_and_b32 v189, s62, v142\n"
    "v_lshlrev_b32 v190, 16, v143\n"
    "v_and_b32 v191, s62, v143\n"
    "v_pk_mul_f32 v[184:185], v[184:185], s[60:61]\n"
    "v_pk_mul_f32 v[186:187], v[186:187], s[60:61]\n"
    "v_pk_mul_f32 v[188:189], v[188:189], s[60:61]\n"
    "v_pk_mul_f32 v[190:191], v[190:191], s[60:61]\n"
    "v_mfma_f32_16x16x32_bf16 v[4:7], a[104:107], v[40:43], v[4:7]\n"
    "v_cvt_pk_bf16_f32 v140, v184, v185\n"
    "v_cvt_pk_bf16_f32 v141, v186, v187\n"
    "v_cvt_pk_bf16_f32 v142, v188, v189\n"
    "v_cvt_pk_bf16_f32 v143, v190, v191\n"
    "v_mfma_f32_16x16x32_bf16 v[4:7], a[108:111], v[44:47], v[4:7]\n"
    "ds_write_b128 v241, v[140:143] offset:3072\n"
    "ds_read_b128 a[112:115], v232 offset:0\n"
    "ds_read_b128 a[116:119], v232 offset:256\n"
    "ds_read_b128 a[120:123], v232 offset:512\n"
    "ds_read_b128 a[124:127], v232 offset:768\n"
    "v_exp_f32 v0, v0\n"
    "v_exp_f32 v1, v1\n"
    "v_exp_f32 v2, v2\n"
    "v_cvt_pk_bf16_f32 v92, v0, v1\n"
    "v_exp_f32 v3, v3\n"
    "s_waitcnt lgkmcnt(3)\n"
    "v_mfma_f32_16x16x32_bf16 v[8:11], a[112:115], v[16:19], -4.0\n"
    "v_cvt_pk_bf16_f32 v93, v2, v3\n"
    "v_exp_f32 v4, v4\n"
    "s_waitcnt lgkmcnt(2)\n"
    "v_mfma_f32_16x16x32_bf16 v[8:11], a[116:119], v[20:23], v[8:11]\n"
    "v_exp_f32 v5, v5\n"
    "s_waitcnt lgkmcnt(1)\n"
    "v_mfma_f32_16x16x32_bf16 v[8:11], a[120:123], v[24:27], v[8:11]\n"
    "v_exp_f32 v6, v6\n"
    "v_cvt_pk_bf16_f32 v108, v4, v5\n"
    "s_waitcnt lgkmcnt(0)\n"
    "v_mfma_f32_16x16x32_bf16 v[8:11], a[124:127], v[28:31], v[8:11]\n"
    "v_exp_f32 v7, v7\n"
    "s_xor_b32 m0, s66, 12288\n"
    "s_mov_b32 s68, 16384\n"
    "buffer_load_dwordx4 v228, s[40:43], s68 offen offset:0 lds\n"
    "buffer_load_dwordx4 v228, s[40:43], s68 offen offset:1024 lds\n"
    "v_mfma_f32_16x16x32_bf16 v[12:15], a[112:115], v[32:35], -4.0\n"
    "ds_read_b128 v[16:19], v221 offset:0\n"
    "v_cvt_pk_bf16_f32 v109, v6, v7\n"
    "v_mfma_f32_16x16x32_bf16 v[12:15], a[116:119], v[36:39], v[12:15]\n"
    "s_add_u32 m0, s81, 4096\n"
    "s_nop 0\n"
    "buffer_load_dwordx4 v231, s[48:51], s70 offen offset:1024 lds\n"
    "ds_read_b128 v[20:23], v221 offset:256\n"
    "v_mfma_f32_16x16x32_bf16 v[12:15], a[120:123], v[40:43], v[12:15]\n"
    "ds_read_b128 v[24:27], v221 offset:512\n"
    "v_mfma_f32_16x16x32_bf16 v[12:15], a[124:127], v[44:47], v[12:15]\n"
    "ds_read_b128 v[28:31], v221 offset:768\n"
    "buffer_load_dwordx4 v231, s[48:51], s70 offen offset:2048 lds\n"
    "ds_read_b128 v[32:35], v221 offset:32768\n"
    "ds_read_b128 v[36:39], v221 offset:33024\n"
    "ds_read_b128 v[40:43], v221 offset:33280\n"
    "ds_read_b128 v[44:47], v221 offset:33536\n"
    "buffer_load_dwordx4 v231, s[48:51], s70 offen offset:3072 lds\n"
    "v_exp_f32 v8, v8\n"
    "v_exp_f32 v9, v9\n"
    "v_exp_f32 v10, v10\n"
    "v_cvt_pk_bf16_f32 v94, v8, v9\n"
    "v_exp_f32 v11, v11\n"
    "s_waitcnt vmcnt(10) lgkmcnt(8)\n"
    "s_barrier\n"
    "s_waitcnt lgkmcnt(4)\n"
    "v_mfma_f32_16x16x32_bf16 v[0:3], a[96:99], v[16:19], -4.0\n"
    "ds_read_b64_tr_b16 a[128:129], v248 offset:8192\n"
    "v_cvt_pk_bf16_f32 v95, v10, v11\n"
    "v_exp_f32 v12, v12\n"
    "v_mfma_f32_16x16x32_bf16 v[0:3], a[100:103], v[20:23], v[0:3]\n"
    "ds_read_b64_tr_b16 a[130:131], v248 offset:12288\n"
    "ds_write_b128 v222, v[92:95] offset:11264\n"
    "v_exp_f32 v13, v13\n"
    "v_mfma_f32_16x16x32_bf16 v[0:3], a[104:107], v[24:27], v[0:3]\n"
    "ds_read_b64_tr_b16 a[136:137], v249 offset:8192\n"
    "v_exp_f32 v14, v14\n"
    "v_cvt_pk_bf16_f32 v110, v12, v13\n"
    "v_mfma_f32_16x16x32_bf16 v[0:3], a[108:111], v[28:31], v[0:3]\n"
    "ds_read_b64_tr_b16 a[138:139], v249 offset:12288\n"
    "v_exp_f32 v15, v15\n"
    "v_mfma_f32_16x16x32_bf16 v[4:7], a[112:115], v[16:19], -4.0\n"
    "ds_read_b64_tr_b16 a[144:145], v250 offset:8192\n"
    "v_cvt_pk_bf16_f32 v111, v14, v15\n"
    "v_mfma_f32_16x16x32_bf16 v[4:7], a[116:119], v[20:23], v[4:7]\n"
    "ds_read_b64_tr_b16 a[146:147], v250 offset:12288\n"
    "ds_write_b128 v222, v[108:111] offset:15360\n"
    "v_mfma_f32_16x16x32_bf16 v[4:7], a[120:123], v[24:27], v[4:7]\n"
    "ds_read_b64_tr_b16 a[152:153], v251 offset:8192\n"
    "v_mfma_f32_16x16x32_bf16 v[4:7], a[124:127], v[28:31], v[4:7]\n"
    "ds_read_b64_tr_b16 a[154:155], v251 offset:12288\n"
    "s_waitcnt lgkmcnt(6)\n"
    "v_mfma_f32_16x16x32_bf16 v[8:11], a[96:99], v[32:35], -4.0\n"
    "ds_read_b64_tr_b16 a[132:133], v248 offset:8200\n"
    "v_exp_f32 v0, v0\n"
    "v_mfma_f32_16x16x32_bf16 v[8:11], a[100:103], v[36:39], v[8:11]\n"
    "ds_read_b64_tr_b16 a[134:135], v248 offset:12296\n"
    "v_exp_f32 v1, v1\n"
    "v_mfma_f32_16x16x32_bf16 v[8:11], a[104:107], v[40:43], v[8:11]\n"
    "ds_read_b64_tr_b16 a[140:141], v249 offset:8200\n"
    "v_exp_f32 v2, v2\n"
    "v_cvt_pk_bf16_f32 v60, v0, v1\n"
    "v_mfma_f32_16x16x32_bf16 v[8:11], a[108:111], v[44:47], v[8:11]\n"
    "ds_read_b64_tr_b16 a[142:143], v249 offset:12296\n"
    "v_exp_f32 v3, v3\n"
    "v_mfma_f32_16x16x32_bf16 v[12:15], a[112:115], v[32:35], -4.0\n"
    "ds_read_b64_tr_b16 a[148:149], v250 offset:8200\n"
    "v_cvt_pk_bf16_f32 v61, v2, v3\n"
    "v_exp_f32 v4, v4\n"
    "v_mfma_f32_16x16x32_bf16 v[12:15], a[116:119], v[36:39], v[12:15]\n"
    "ds_read_b64_tr_b16 a[150:151], v250 offset:12296\n"
    "v_exp_f32 v5, v5\n"
    "v_mfma_f32_16x16x32_bf16 v[12:15], a[120:123], v[40:43], v[12:15]\n"
    "ds_read_b64_tr_b16 a[156:157], v251 offset:8200\n"
    "v_exp_f32 v6, v6\n"
    "v_cvt_pk_bf16_f32 v62, v4, v5\n"
    "v_mfma_f32_16x16x32_bf16 v[12:15], a[124:127], v[44:47], v[12:15]\n"
    "ds_read_b64_tr_b16 a[158:159], v251 offset:12296\n"
    "v_exp_f32 v7, v7\n"
    "v_exp_f32 v8, v8\n"
    "v_cvt_pk_bf16_f32 v63, v6, v7\n"
    "v_exp_f32 v9, v9\n"
    "ds_write_b128 v222, v[60:63] offset:3072\n"
    "v_exp_f32 v10, v10\n"
    "v_cvt_pk_bf16_f32 v76, v8, v9\n"
    "v_exp_f32 v11, v11\n"
    "v_exp_f32 v12, v12\n"
    "v_cvt_pk_bf16_f32 v77, v10, v11\n"
    "v_exp_f32 v13, v13\n"
    "v_exp_f32 v14, v14\n"
    "v_cvt_pk_bf16_f32 v78, v12, v13\n"
    "v_exp_f32 v15, v15\n"
    "s_nop 0\n"
    "v_cvt_pk_bf16_f32 v79, v14, v15\n"
    "s_waitcnt lgkmcnt(1)\n"
    "ds_write_b128 v222, v[76:79] offset:7168\n"
    "s_waitcnt lgkmcnt(0)\n"
    "s_barrier\n"
    "v_mfma_f32_16x16x32_bf16 v[252:255], v[204:207], v[48:51], 0\n"
    "s_xor_b32 m0, s66, 4096\n"
    "s_xor_b32 s70, s72, 16384\n"
    "buffer_load_dwordx4 v231, s[48:51], s70 offen offset:0 lds\n"
    "v_mfma_f32_16x16x32_bf16 v[252:255], v[204:207], v[52:55], v[252:255]\n"
    "ds_read_b128 v[112:115], v223 offset:0\n"
    "v_mfma_f32_16x16x32_bf16 v[252:255], v[204:207], v[56:59], v[252:255]\n"
    "v_mfma_f32_16x16x32_bf16 v[252:255], v[204:207], v[60:63], v[252:255]\n"
    "ds_read_b128 v[116:119], v224 offset:0\n"
    "v_mfma_f32_16x16x32_bf16 v[252:255], v[208:211], v[64:67], v[252:255]\n"
    "buffer_load_dwordx4 v231, s[48:51], s70 offen offset:1024 lds\n"
    "v_mfma_f32_16x16x32_bf16 v[252:255], v[208:211], v[68:71], v[252:255]\n"
    "ds_read_b128 v[120:123], v225 offset:0\n"
    "v_mfma_f32_16x16x32_bf16 v[252:255], v[208:211], v[72:75], v[252:255]\n"
    "v_mfma_f32_16x16x32_bf16 v[252:255], v[208:211], v[76:79], v[252:255]\n"
    "ds_read_b128 v[124:127], v223 offset:4096\n"
    "v_mfma_f32_16x16x32_bf16 v[200:203], v[204:207], v[80:83], 0\n"
    "buffer_load_dwordx4 v231, s[48:51], s70 offen offset:2048 lds\n"
    "v_mfma_f32_16x16x32_bf16 v[200:203], v[204:207], v[84:87], v[200:203]\n"
    "ds_read_b128 v[128:131], v224 offset:4096\n"
    "v_mfma_f32_16x16x32_bf16 v[200:203], v[204:207], v[88:91], v[200:203]\n"
    "v_mfma_f32_16x16x32_bf16 v[200:203], v[204:207], v[92:95], v[200:203]\n"
    "ds_read_b128 v[132:135], v225 offset:4096\n"
    "v_mfma_f32_16x16x32_bf16 v[200:203], v[208:211], v[96:99], v[200:203]\n"
    "buffer_load_dwordx4 v231, s[48:51], s70 offen offset:3072 lds\n"
    "v_mfma_f32_16x16x32_bf16 v[200:203], v[208:211], v[100:103], v[200:203]\n"
    "ds_read_b128 v[136:139], v223 offset:8192\n"
    "v_mfma_f32_16x16x32_bf16 v[200:203], v[208:211], v[104:107], v[200:203]\n"
    "v_mfma_f32_16x16x32_bf16 v[200:203], v[208:211], v[108:111], v[200:203]\n"
    "ds_read_b128 v[140:143], v224 offset:8192\n"
    "v_mfma_f32_16x16x32_bf16 v[160:163], a[128:131], v[48:51], 0\n"
    "s_xor_b32 m0, s66, 8192\n"
    "s_xor_b32 s70, s72, 20480\n"
    "buffer_load_dwordx4 v231, s[48:51], s70 offen offset:0 lds\n"
    "s_waitcnt lgkmcnt(2)\n"
    "v_mfma_f32_16x16x32_bf16 v[160:163], a[136:139], v[112:115], v[160:163]\n"
    "ds_read_b128 v[144:147], v225 offset:8192\n"
    "v_mfma_f32_16x16x32_bf16 v[160:163], a[144:147], v[116:119], v[160:163]\n"
    "v_mfma_f32_16x16x32_bf16 v[160:163], a[152:155], v[120:123], v[160:163]\n"
    "ds_read_b128 v[148:151], v223 offset:12288\n"
    "v_mfma_f32_16x16x32_bf16 v[164:167], a[132:135], v[48:51], 0\n"
    "buffer_load_dwordx4 v231, s[48:51], s70 offen offset:1024 lds\n"
    "v_mfma_f32_16x16x32_bf16 v[164:167], a[140:143], v[112:115], v[164:167]\n"
    "ds_read_b128 v[152:155], v224 offset:12288\n"
    "v_mfma_f32_16x16x32_bf16 v[164:167], a[148:151], v[116:119], v[164:167]\n"
    "v_mfma_f32_16x16x32_bf16 v[164:167], a[156:159], v[120:123], v[164:167]\n"
    "ds_read_b128 v[156:159], v225 offset:12288\n"
    "v_mfma_f32_16x16x32_bf16 v[168:171], a[128:131], v[64:67], 0\n"
    "buffer_load_dwordx4 v231, s[48:51], s70 offen offset:2048 lds\n"
    "v_mfma_f32_16x16x32_bf16 v[168:171], a[136:139], v[124:127], v[168:171]\n"
    "ds_write_b32 v243, v252 offset:0\n"
    "v_mfma_f32_16x16x32_bf16 v[168:171], a[144:147], v[128:131], v[168:171]\n"
    "v_mfma_f32_16x16x32_bf16 v[168:171], a[152:155], v[132:135], v[168:171]\n"
    "v_mfma_f32_16x16x32_bf16 v[172:175], a[132:135], v[64:67], 0\n"
    "buffer_load_dwordx4 v231, s[48:51], s70 offen offset:3072 lds\n"
    "v_mfma_f32_16x16x32_bf16 v[172:175], a[140:143], v[124:127], v[172:175]\n"
    "v_mfma_f32_16x16x32_bf16 v[172:175], a[148:151], v[128:131], v[172:175]\n"
    "v_mfma_f32_16x16x32_bf16 v[172:175], a[156:159], v[132:135], v[172:175]\n"
    "s_waitcnt vmcnt(8) lgkmcnt(0)\n"
    "s_barrier\n"
    "v_mfma_f32_16x16x32_bf16 v[176:179], a[128:131], v[80:83], 0\n"
    "ds_read_b128 v[16:19], v221 offset:4096\n"
    "ds_read_b64_tr_b16 a[160:161], v236 offset:0\n"
    "v_mfma_f32_16x16x32_bf16 v[176:179], a[136:139], v[136:139], v[176:179]\n"
    "ds_read_b64_tr_b16 a[162:163], v236 offset:4096\n"
    "v_mfma_f32_16x16x32_bf16 v[176:179], a[144:147], v[140:143], v[176:179]\n"
    "ds_read_b128 v[20:23], v221 offset:4352\n"
    "ds_read_b64_tr_b16 a[168:169], v237 offset:0\n"
    "v_mfma_f32_16x16x32_bf16 v[176:179], a[152:155], v[144:147], v[176:179]\n"
    "ds_read_b64_tr_b16 a[170:171], v237 offset:4096\n"
    "v_mfma_f32_16x16x32_bf16 v[180:183], a[132:135], v[80:83], 0\n"
    "ds_read_b128 v[24:27], v221 offset:4608\n"
    "ds_read_b64_tr_b16 a[176:177], v238 offset:0\n"
    "v_mfma_f32_16x16x32_bf16 v[180:183], a[140:143], v[136:139], v[180:183]\n"
    "ds_read_b64_tr_b16 a[178:179], v238 offset:4096\n"
    "v_mfma_f32_16x16x32_bf16 v[180:183], a[148:151], v[140:143], v[180:183]\n"
    "ds_read_b128 v[28:31], v221 offset:4864\n"
    "ds_read_b64_tr_b16 a[184:185], v239 offset:0\n"
    "v_mfma_f32_16x16x32_bf16 v[180:183], a[156:159], v[144:147], v[180:183]\n"
    "ds_read_b64_tr_b16 a[186:187], v239 offset:4096\n"
    "v_mfma_f32_16x16x32_bf16 v[184:187], a[128:131], v[96:99], 0\n"
    "ds_read_b128 v[212:215], v244 offset:0\n"
    "ds_read_b64_tr_b16 a[164:165], v236 offset:8\n"
    "v_mfma_f32_16x16x32_bf16 v[184:187], a[136:139], v[148:151], v[184:187]\n"
    "ds_read_b64_tr_b16 a[166:167], v236 offset:4104\n"
    "v_mfma_f32_16x16x32_bf16 v[184:187], a[144:147], v[152:155], v[184:187]\n"
    "s_waitcnt lgkmcnt(7)\n"
    "ds_read_b128 v[32:35], v221 offset:36864\n"
    "ds_read_b64_tr_b16 a[172:173], v237 offset:8\n"
    "v_mfma_f32_16x16x32_bf16 v[184:187], a[152:155], v[156:159], v[184:187]\n"
    "ds_read_b64_tr_b16 a[174:175], v237 offset:4104\n"
    "v_mfma_f32_16x16x32_bf16 v[188:191], a[132:135], v[96:99], 0\n"
    "ds_read_b128 v[36:39], v221 offset:37120\n"
    "ds_read_b64_tr_b16 a[180:181], v238 offset:8\n"
    "v_mfma_f32_16x16x32_bf16 v[188:191], a[140:143], v[148:151], v[188:191]\n"
    "ds_read_b64_tr_b16 a[182:183], v238 offset:4104\n"
    "v_mfma_f32_16x16x32_bf16 v[188:191], a[148:151], v[152:155], v[188:191]\n"
    "ds_read_b128 v[40:43], v221 offset:37376\n"
    "ds_read_b64_tr_b16 a[188:189], v239 offset:8\n"
    "v_mfma_f32_16x16x32_bf16 v[188:191], a[156:159], v[156:159], v[188:191]\n"
    "s_waitcnt lgkmcnt(7)\n"
    "ds_read_b64_tr_b16 a[190:191], v239 offset:4104\n"
    "v_mfma_f32_16x16x32_bf16 v[0:3], a[0:3], v[16:19], -4.0\n"
    "ds_read_b128 v[44:47], v221 offset:37632\n"
    "v_mfma_f32_16x16x32_bf16 v[0:3], a[4:7], v[20:23], v[0:3]\n"
    "ds_read_b128 v[112:115], v223 offset:1024\n"
    "v_mfma_f32_16x16x32_bf16 v[0:3], a[8:11], v[24:27], v[0:3]\n"
    "ds_read_b128 v[116:119], v224 offset:1024\n"
    "v_mfma_f32_16x16x32_bf16 v[0:3], a[12:15], v[28:31], v[0:3]\n"
    "ds_read_b128 v[120:123], v225 offset:1024\n"
    "s_waitcnt lgkmcnt(4)\n"
    "s_waitcnt lgkmcnt(4)\n"
    "s_barrier\n"
    "v_mfma_f32_16x16x32_bf16 v[4:7], a[16:19], v[16:19], -4.0\n"
    "s_xor_b32 m0, s66, 14336\n"
    "s_mov_b32 s68, 24576\n"
    "buffer_load_dwordx4 v228, s[40:43], s68 offen offset:0 lds\n"
    "ds_read_b128 v[124:127], v223 offset:5120\n"
    "v_mfma_f32_16x16x32_bf16 v[4:7], a[20:23], v[20:23], v[4:7]\n"
    "ds_read_b128 v[128:131], v224 offset:5120\n"
    "v_mfma_f32_16x16x32_bf16 v[4:7], a[24:27], v[24:27], v[4:7]\n"
    "ds_read_b128 v[132:135], v225 offset:5120\n"
    "v_mfma_f32_16x16x32_bf16 v[4:7], a[28:31], v[28:31], v[4:7]\n"
    "v_mfma_f32_16x16x32_bf16 v[8:11], a[0:3], v[32:35], -4.0\n"
    "buffer_load_dwordx4 v228, s[40:43], s68 offen offset:1024 lds\n"
    "v_exp_f32 v0, v0\n"
    "v_mfma_f32_16x16x32_bf16 v[8:11], a[4:7], v[36:39], v[8:11]\n"
    "v_exp_f32 v1, v1\n"
    "v_mfma_f32_16x16x32_bf16 v[8:11], a[8:11], v[40:43], v[8:11]\n"
    "v_exp_f32 v2, v2\n"
    "v_cvt_pk_bf16_f32 v48, v0, v1\n"
    "s_waitcnt lgkmcnt(1)\n"
    "v_mfma_f32_16x16x32_bf16 v[8:11], a[12:15], v[44:47], v[8:11]\n"
    "v_exp_f32 v3, v3\n"
    "v_mfma_f32_16x16x32_bf16 v[12:15], a[16:19], v[32:35], -4.0\n"
    "s_add_u32 m0, s81, 0\n"
    "s_xor_b32 s70, s72, 24576\n"
    "buffer_load_dwordx4 v231, s[48:51], s70 offen offset:0 lds\n"
    "v_cvt_pk_bf16_f32 v49, v2, v3\n"
    "v_exp_f32 v4, v4\n"
    "v_mfma_f32_16x16x32_bf16 v[12:15], a[20:23], v[36:39], v[12:15]\n"
    "v_exp_f32 v5, v5\n"
    "v_mfma_f32_16x16x32_bf16 v[12:15], a[24:27], v[40:43], v[12:15]\n"
    "v_exp_f32 v6, v6\n"
    "v_cvt_pk_bf16_f32 v50, v4, v5\n"
    "v_mfma_f32_16x16x32_bf16 v[12:15], a[28:31], v[44:47], v[12:15]\n"
    "v_exp_f32 v7, v7\n"
    "v_mfma_f32_16x16x32_bf16 v[160:163], a[160:163], v[52:55], v[160:163]\n"
    "buffer_load_dwordx4 v231, s[48:51], s70 offen offset:1024 lds\n"
    "v_exp_f32 v8, v8\n"
    "v_mfma_f32_16x16x32_bf16 v[160:163], a[168:171], v[112:115], v[160:163]\n"
    "ds_read_b128 v[136:139], v223 offset:9216\n"
    "v_cvt_pk_bf16_f32 v51, v6, v7\n"
    "v_exp_f32 v9, v9\n"
    "v_mfma_f32_16x16x32_bf16 v[160:163], a[176:179], v[116:119], v[160:163]\n"
    "ds_write_b128 v222, v[48:51] offset:0\n"
    "v_mfma_f32_16x16x32_bf16 v[160:163], a[184:187], v[120:123], v[160:163]\n"
    "ds_read_b128 v[140:143], v224 offset:9216\n"
    "v_exp_f32 v10, v10\n"
    "v_mfma_f32_16x16x32_bf16 v[164:167], a[164:167], v[52:55], v[164:167]\n"
    "buffer_load_dwordx4 v231, s[48:51], s70 offen offset:2048 lds\n"
    "v_cvt_pk_bf16_f32 v64, v8, v9\n"
    "v_exp_f32 v11, v11\n"
    "v_mfma_f32_16x16x32_bf16 v[164:167], a[172:175], v[112:115], v[164:167]\n"
    "ds_read_b128 v[144:147], v225 offset:9216\n"
    "v_exp_f32 v12, v12\n"
    "v_mfma_f32_16x16x32_bf16 v[164:167], a[180:183], v[116:119], v[164:167]\n"
    "v_cvt_pk_bf16_f32 v65, v10, v11\n"
    "v_exp_f32 v13, v13\n"
    "v_mfma_f32_16x16x32_bf16 v[164:167], a[188:191], v[120:123], v[164:167]\n"
    "ds_read_b128 v[148:151], v223 offset:13312\n"
    "v_exp_f32 v14, v14\n"
    "v_mfma_f32_16x16x32_bf16 v[168:171], a[160:163], v[68:71], v[168:171]\n"
    "buffer_load_dwordx4 v231, s[48:51], s70 offen offset:3072 lds\n"
    "v_cvt_pk_bf16_f32 v66, v12, v13\n"
    "v_exp_f32 v15, v15\n"
    "v_mfma_f32_16x16x32_bf16 v[168:171], a[168:171], v[124:127], v[168:171]\n"
    "ds_read_b128 v[152:155], v224 offset:13312\n"
    "v_cvt_pk_bf16_f32 v67, v14, v15\n"
    "ds_write_b128 v222, v[64:67] offset:4096\n"
    "v_mfma_f32_16x16x32_bf16 v[168:171], a[176:179], v[128:131], v[168:171]\n"
    "v_add_f32 v218, v212, v213\n"
    "v_add_f32 v219, v214, v215\n"
    "s_waitcnt lgkmcnt(3)\n"
    "v_mfma_f32_16x16x32_bf16 v[168:171], a[184:187], v[132:135], v[168:171]\n"
    "ds_read_b128 v[156:159], v225 offset:13312\n"
    "v_add_f32 v218, v218, v219\n"
    "v_rcp_f32 v216, v218\n"
    "v_mfma_f32_16x16x32_bf16 v[172:175], a[164:167], v[68:71], v[172:175]\n"
    "s_add_u32 m0, s81, 4096\n"
    "s_xor_b32 s70, s72, 28672\n"
    "buffer_load_dwordx4 v231, s[48:51], s70 offen offset:0 lds\n"
    "v_subrev_u32 v219, s63, v218\n"
    "v_mov_b32 v217, v216\n"
    "v_mfma_f32_16x16x32_bf16 v[172:175], a[172:175], v[124:127], v[172:175]\n"
    "v_cmp_le_u32 vcc, s64, v219\n"
    "v_permlane16_swap_b32 v216, v217\n"
    "v_mfma_f32_16x16x32_bf16 v[172:175], a[180:183], v[128:131], v[172:175]\n"
    "v_mfma_f32_16x16x32_bf16 v[172:175], a[188:191], v[132:135], v[172:175]\n"
    "v_mfma_f32_16x16x32_bf16 v[176:179], a[160:163], v[84:87], v[176:179]\n"
    "buffer_load_dwordx4 v231, s[48:51], s70 offen offset:1024 lds\n"
    "v_mfma_f32_16x16x32_bf16 v[176:179], a[168:171], v[136:139], v[176:179]\n"
    "ds_read_b128 v[112:115], v223 offset:2048\n"
    "v_mfma_f32_16x16x32_bf16 v[176:179], a[176:179], v[140:143], v[176:179]\n"
    "v_mfma_f32_16x16x32_bf16 v[176:179], a[184:187], v[144:147], v[176:179]\n"
    "ds_read_b128 v[116:119], v224 offset:2048\n"
    "v_mfma_f32_16x16x32_bf16 v[180:183], a[164:167], v[84:87], v[180:183]\n"
    "buffer_load_dwordx4 v231, s[48:51], s70 offen offset:2048 lds\n"
    "v_mfma_f32_16x16x32_bf16 v[180:183], a[172:175], v[136:139], v[180:183]\n"
    "ds_read_b128 v[120:123], v225 offset:2048\n"
    "v_mfma_f32_16x16x32_bf16 v[180:183], a[180:183], v[140:143], v[180:183]\n"
    "v_mfma_f32_16x16x32_bf16 v[180:183], a[188:191], v[144:147], v[180:183]\n"
    "ds_read_b128 v[124:127], v223 offset:6144\n"
    "v_mfma_f32_16x16x32_bf16 v[184:187], a[160:163], v[100:103], v[184:187]\n"
    "buffer_load_dwordx4 v231, s[48:51], s70 offen offset:3072 lds\n"
    "s_waitcnt lgkmcnt(2)\n"
    "v_mfma_f32_16x16x32_bf16 v[184:187], a[168:171], v[148:151], v[184:187]\n"
    "ds_read_b128 v[128:131], v224 offset:6144\n"
    "v_mfma_f32_16x16x32_bf16 v[184:187], a[176:179], v[152:155], v[184:187]\n"
    "v_mfma_f32_16x16x32_bf16 v[184:187], a[184:187], v[156:159], v[184:187]\n"
    "ds_read_b128 v[132:135], v225 offset:6144\n"
    "v_mfma_f32_16x16x32_bf16 v[188:191], a[164:167], v[100:103], v[188:191]\n"
    "v_mfma_f32_16x16x32_bf16 v[188:191], a[172:175], v[148:151], v[188:191]\n"
    "v_mfma_f32_16x16x32_bf16 v[188:191], a[180:183], v[152:155], v[188:191]\n"
    "v_mfma_f32_16x16x32_bf16 v[188:191], a[188:191], v[156:159], v[188:191]\n"
    "s_waitcnt vmcnt(10) lgkmcnt(4)\n"
    "s_barrier\n"
    "v_mfma_f32_16x16x32_bf16 v[0:3], a[32:35], v[16:19], -4.0\n"
    "ds_read_b64_tr_b16 a[192:193], v248 offset:12288\n"
    "v_mfma_f32_16x16x32_bf16 v[0:3], a[36:39], v[20:23], v[0:3]\n"
    "ds_read_b64_tr_b16 a[194:195], v248 offset:0\n"
    "v_mfma_f32_16x16x32_bf16 v[0:3], a[40:43], v[24:27], v[0:3]\n"
    "ds_read_b64_tr_b16 a[200:201], v249 offset:12288\n"
    "v_mfma_f32_16x16x32_bf16 v[0:3], a[44:47], v[28:31], v[0:3]\n"
    "ds_read_b64_tr_b16 a[202:203], v249 offset:0\n"
    "v_mfma_f32_16x16x32_bf16 v[4:7], a[48:51], v[16:19], -4.0\n"
    "ds_read_b64_tr_b16 a[208:209], v250 offset:12288\n"
    "v_mfma_f32_16x16x32_bf16 v[4:7], a[52:55], v[20:23], v[4:7]\n"
    "ds_read_b64_tr_b16 a[210:211], v250 offset:0\n"
    "v_mfma_f32_16x16x32_bf16 v[4:7], a[56:59], v[24:27], v[4:7]\n"
    "ds_read_b64_tr_b16 a[216:217], v251 offset:12288\n"
    "v_mfma_f32_16x16x32_bf16 v[4:7], a[60:63], v[28:31], v[4:7]\n"
    "ds_read_b64_tr_b16 a[218:219], v251 offset:0\n"
    "v_mfma_f32_16x16x32_bf16 v[8:11], a[32:35], v[32:35], -4.0\n"
    "ds_read_b64_tr_b16 a[196:197], v248 offset:12296\n"
    "v_exp_f32 v0, v0\n"
    "v_mfma_f32_16x16x32_bf16 v[8:11], a[36:39], v[36:39], v[8:11]\n"
    "ds_read_b64_tr_b16 a[198:199], v248 offset:8\n"
    "v_exp_f32 v1, v1\n"
    "v_mfma_f32_16x16x32_bf16 v[8:11], a[40:43], v[40:43], v[8:11]\n"
    "ds_read_b64_tr_b16 a[204:205], v249 offset:12296\n"
    "v_exp_f32 v2, v2\n"
    "v_cvt_pk_bf16_f32 v52, v0, v1\n"
    "v_mfma_f32_16x16x32_bf16 v[8:11], a[44:47], v[44:47], v[8:11]\n"
    "s_waitcnt lgkmcnt(5)\n"
    "ds_read_b64_tr_b16 a[206:207], v249 offset:8\n"
    "v_exp_f32 v3, v3\n"
    "v_mfma_f32_16x16x32_bf16 v[12:15], a[48:51], v[32:35], -4.0\n"
    "ds_read_b64_tr_b16 a[212:213], v250 offset:12296\n"
    "v_cvt_pk_bf16_f32 v53, v2, v3\n"
    "v_exp_f32 v4, v4\n"
    "v_mfma_f32_16x16x32_bf16 v[12:15], a[52:55], v[36:39], v[12:15]\n"
    "ds_read_b64_tr_b16 a[214:215], v250 offset:8\n"
    "v_exp_f32 v5, v5\n"
    "v_mfma_f32_16x16x32_bf16 v[12:15], a[56:59], v[40:43], v[12:15]\n"
    "ds_read_b64_tr_b16 a[220:221], v251 offset:12296\n"
    "v_exp_f32 v6, v6\n"
    "v_cvt_pk_bf16_f32 v54, v4, v5\n"
    "v_mfma_f32_16x16x32_bf16 v[12:15], a[60:63], v[44:47], v[12:15]\n"
    "ds_read_b64_tr_b16 a[222:223], v251 offset:8\n"
    "v_exp_f32 v7, v7\n"
    "v_mfma_f32_16x16x32_bf16 v[160:163], a[192:195], v[56:59], v[160:163]\n"
    "s_mov_b32 s46, 0\n"
    "s_xor_b32 m0, s66, 0\n"
    "s_mov_b32 s68, 32768\n"
    "buffer_load_dwordx4 v228, s[40:43], s68 offen offset:0 lds\n"
    "v_exp_f32 v8, v8\n"
    "v_mfma_f32_16x16x32_bf16 v[160:163], a[200:203], v[112:115], v[160:163]\n"
    "ds_read_b128 v[136:139], v223 offset:10240\n"
    "v_cvt_pk_bf16_f32 v55, v6, v7\n"
    "v_exp_f32 v9, v9\n"
    "v_mfma_f32_16x16x32_bf16 v[160:163], a[208:211], v[116:119], v[160:163]\n"
    "ds_write_b128 v222, v[52:55] offset:1024\n"
    "s_waitcnt lgkmcnt(2)\n"
    "v_mfma_f32_16x16x32_bf16 v[160:163], a[216:219], v[120:123], v[160:163]\n"
    "ds_read_b128 v[140:143], v224 offset:10240\n"
    "v_exp_f32 v10, v10\n"
    "v_mfma_f32_16x16x32_bf16 v[164:167], a[196:199], v[56:59], v[164:167]\n"
    "buffer_load_dwordx4 v228, s[40:43], s68 offen offset:1024 lds\n"
    "v_cvt_pk_bf16_f32 v68, v8, v9\n"
    "v_exp_f32 v11, v11\n"
    "v_mfma_f32_16x16x32_bf16 v[164:167], a[204:207], v[112:115], v[164:167]\n"
    "ds_read_b128 v[144:147], v225 offset:10240\n"
    "v_exp_f32 v12, v12\n"
    "v_mfma_f32_16x16x32_bf16 v[164:167], a[212:215], v[116:119], v[164:167]\n"
    "v_cvt_pk_bf16_f32 v69, v10, v11\n"
    "v_exp_f32 v13, v13\n"
    "v_mfma_f32_16x16x32_bf16 v[164:167], a[220:223], v[120:123], v[164:167]\n"
    "ds_read_b128 v[148:151], v223 offset:14336\n"
    "v_exp_f32 v14, v14\n"
    "v_mfma_f32_16x16x32_bf16 v[168:171], a[192:195], v[72:75], v[168:171]\n"
    "buffer_load_dword v242, off, s[44:47], 0\n"
    "v_cvt_pk_bf16_f32 v70, v12, v13\n"
    "v_exp_f32 v15, v15\n"
    "v_mfma_f32_16x16x32_bf16 v[168:171], a[200:203], v[124:127], v[168:171]\n"
    "ds_read_b128 v[152:155], v224 offset:14336\n"
    "v_cvt_pk_bf16_f32 v71, v14, v15\n"
    "ds_write_b128 v222, v[68:71] offset:5120\n"
    "v_mfma_f32_16x16x32_bf16 v[168:171], a[208:211], v[128:131], v[168:171]\n"
    "v_mov_b32 v192, v60\n"
    "v_mov_b32 v193, v61\n"
    "v_mfma_f32_16x16x32_bf16 v[168:171], a[216:219], v[132:135], v[168:171]\n"
    "ds_read_b128 v[156:159], v225 offset:14336\n"
    "v_mov_b32 v194, v62\n"
    "v_mov_b32 v195, v63\n"
    "v_mfma_f32_16x16x32_bf16 v[172:175], a[196:199], v[72:75], v[172:175]\n"
    "buffer_load_dword v242, off, s[44:47], 0\n"
    "v_mov_b32 v196, v76\n"
    "v_mov_b32 v197, v77\n"
    "v_mfma_f32_16x16x32_bf16 v[172:175], a[204:207], v[124:127], v[172:175]\n"
    "v_mov_b32 v198, v78\n"
    "v_mov_b32 v199, v79\n"
    "v_mfma_f32_16x16x32_bf16 v[172:175], a[212:215], v[128:131], v[172:175]\n"
    "v_mfma_f32_16x16x32_bf16 v[172:175], a[220:223], v[132:135], v[172:175]\n"
    "v_mfma_f32_16x16x32_bf16 v[176:179], a[192:195], v[88:91], v[176:179]\n"
    "s_xor_b32 m0, s66, 2048\n"
    "s_mov_b32 s68, 40960\n"
    "buffer_load_dwordx4 v228, s[40:43], s68 offen offset:0 lds\n"
    "ds_read_b128 v[112:115], v223 offset:3072\n"
    "s_waitcnt lgkmcnt(1)\n"
    "v_mfma_f32_16x16x32_bf16 v[176:179], a[200:203], v[136:139], v[176:179]\n"
    "ds_read_b128 v[116:119], v224 offset:3072\n"
    "v_mfma_f32_16x16x32_bf16 v[176:179], a[208:211], v[140:143], v[176:179]\n"
    "ds_read_b128 v[120:123], v225 offset:3072\n"
    "v_mfma_f32_16x16x32_bf16 v[176:179], a[216:219], v[144:147], v[176:179]\n"
    "ds_read_b128 v[124:127], v223 offset:7168\n"
    "v_mfma_f32_16x16x32_bf16 v[180:183], a[196:199], v[88:91], v[180:183]\n"
    "buffer_load_dwordx4 v228, s[40:43], s68 offen offset:1024 lds\n"
    "ds_read_b128 v[128:131], v224 offset:7168\n"
    "v_mfma_f32_16x16x32_bf16 v[180:183], a[204:207], v[136:139], v[180:183]\n"
    "ds_read_b128 v[132:135], v225 offset:7168\n"
    "v_mfma_f32_16x16x32_bf16 v[180:183], a[212:215], v[140:143], v[180:183]\n"
    "v_mfma_f32_16x16x32_bf16 v[180:183], a[220:223], v[144:147], v[180:183]\n"
    "v_mfma_f32_16x16x32_bf16 v[184:187], a[192:195], v[104:107], v[184:187]\n"
    "v_mfma_f32_16x16x32_bf16 v[184:187], a[200:203], v[148:151], v[184:187]\n"
    "s_waitcnt lgkmcnt(0)\n"
    "s_waitcnt vmcnt(14) lgkmcnt(0)\n"
    "s_barrier\n"
    "v_mfma_f32_16x16x32_bf16 v[184:187], a[208:211], v[152:155], v[184:187]\n"
    "buffer_load_dword v242, off, s[44:47], 0\n"
    "v_mfma_f32_16x16x32_bf16 v[184:187], a[216:219], v[156:159], v[184:187]\n"
    "v_mfma_f32_16x16x32_bf16 v[188:191], a[196:199], v[104:107], v[188:191]\n"
    "v_mfma_f32_16x16x32_bf16 v[188:191], a[204:207], v[148:151], v[188:191]\n"
    "v_mfma_f32_16x16x32_bf16 v[188:191], a[212:215], v[152:155], v[188:191]\n"
    "buffer_load_dword v242, off, s[44:47], 0\n"
    "v_mfma_f32_16x16x32_bf16 v[188:191], a[220:223], v[156:159], v[188:191]\n"
    "v_mfma_f32_16x16x32_bf16 v[0:3], a[64:67], v[16:19], -4.0\n"
    "ds_read_b128 v[136:139], v223 offset:11264\n"
    "v_mfma_f32_16x16x32_bf16 v[0:3], a[68:71], v[20:23], v[0:3]\n"
    "ds_read_b128 v[140:143], v224 offset:11264\n"
    "v_mfma_f32_16x16x32_bf16 v[0:3], a[72:75], v[24:27], v[0:3]\n"
    "s_xor_b32 m0, s66, 4096\n"
    "s_mov_b32 s68, 49152\n"
    "buffer_load_dwordx4 v228, s[40:43], s68 offen offset:0 lds\n"
    "ds_read_b128 v[144:147], v225 offset:11264\n"
    "v_mfma_f32_16x16x32_bf16 v[0:3], a[76:79], v[28:31], v[0:3]\n"
    "ds_read_b128 v[148:151], v223 offset:15360\n"
    "v_mfma_f32_16x16x32_bf16 v[4:7], a[80:83], v[16:19], -4.0\n"
    "ds_read_b128 v[152:155], v224 offset:15360\n"
    "v_mfma_f32_16x16x32_bf16 v[4:7], a[84:87], v[20:23], v[4:7]\n"
    "ds_read_b128 v[156:159], v225 offset:15360\n"
    "v_mfma_f32_16x16x32_bf16 v[4:7], a[88:91], v[24:27], v[4:7]\n"
    "buffer_load_dwordx4 v228, s[40:43], s68 offen offset:1024 lds\n"
    "v_mfma_f32_16x16x32_bf16 v[4:7], a[92:95], v[28:31], v[4:7]\n"
    "v_mfma_f32_16x16x32_bf16 v[8:11], a[96:99], v[16:19], -4.0\n"
    "v_exp_f32 v0, v0\n"
    "v_mfma_f32_16x16x32_bf16 v[8:11], a[100:103], v[20:23], v[8:11]\n"
    "v_exp_f32 v1, v1\n"
    "v_mfma_f32_16x16x32_bf16 v[8:11], a[104:107], v[24:27], v[8:11]\n"
    "buffer_load_dword v242, off, s[44:47], 0\n"
    "v_exp_f32 v2, v2\n"
    "v_cvt_pk_bf16_f32 v56, v0, v1\n"
    "v_mfma_f32_16x16x32_bf16 v[8:11], a[108:111], v[28:31], v[8:11]\n"
    "v_exp_f32 v3, v3\n"
    "v_mfma_f32_16x16x32_bf16 v[12:15], a[112:115], v[16:19], -4.0\n"
    "v_cvt_pk_bf16_f32 v57, v2, v3\n"
    "v_exp_f32 v4, v4\n"
    "v_mfma_f32_16x16x32_bf16 v[12:15], a[116:119], v[20:23], v[12:15]\n"
    "v_exp_f32 v5, v5\n"
    "v_mfma_f32_16x16x32_bf16 v[12:15], a[120:123], v[24:27], v[12:15]\n"
    "buffer_load_dword v242, off, s[44:47], 0\n"
    "v_exp_f32 v6, v6\n"
    "v_cvt_pk_bf16_f32 v58, v4, v5\n"
    "v_mfma_f32_16x16x32_bf16 v[12:15], a[124:127], v[28:31], v[12:15]\n"
    "v_exp_f32 v7, v7\n"
    "v_mfma_f32_16x16x32_bf16 v[0:3], a[64:67], v[32:35], -4.0\n"
    "ds_read_b128 v[16:19], v221 offset:6144\n"
    "v_cvt_pk_bf16_f32 v59, v6, v7\n"
    "v_exp_f32 v8, v8\n"
    "v_mfma_f32_16x16x32_bf16 v[0:3], a[68:71], v[36:39], v[0:3]\n"
    "ds_write_b128 v222, v[56:59] offset:2048\n"
    "v_exp_f32 v9, v9\n"
    "v_mfma_f32_16x16x32_bf16 v[0:3], a[72:75], v[40:43], v[0:3]\n"
    "s_xor_b32 m0, s66, 6144\n"
    "s_mov_b32 s68, 57344\n"
    "buffer_load_dwordx4 v228, s[40:43], s68 offen offset:0 lds\n"
    "ds_read_b128 v[20:23], v221 offset:6400\n"
    "v_exp_f32 v10, v10\n"
    "v_cvt_pk_bf16_f32 v60, v8, v9\n"
    "v_mfma_f32_16x16x32_bf16 v[0:3], a[76:79], v[44:47], v[0:3]\n"
    "v_exp_f32 v11, v11\n"
    "v_mfma_f32_16x16x32_bf16 v[4:7], a[80:83], v[32:35], -4.0\n"
    "ds_read_b128 v[24:27], v221 offset:6656\n"
    "v_cvt_pk_bf16_f32 v61, v10, v11\n"
    "v_exp_f32 v12, v12\n"
    "v_mfma_f32_16x16x32_bf16 v[4:7], a[84:87], v[36:39], v[4:7]\n"
    "v_exp_f32 v13, v13\n"
    "v_mfma_f32_16x16x32_bf16 v[4:7], a[88:91], v[40:43], v[4:7]\n"
    "buffer_load_dwordx4 v228, s[40:43], s68 offen offset:1024 lds\n"
    "ds_read_b128 v[28:31], v221 offset:6912\n"
    "v_exp_f32 v14, v14\n"
    "v_cvt_pk_bf16_f32 v62, v12, v13\n"
    "v_mfma_f32_16x16x32_bf16 v[4:7], a[92:95], v[44:47], v[4:7]\n"
    "v_exp_f32 v15, v15\n"
    "v_mfma_f32_16x16x32_bf16 v[8:11], a[96:99], v[32:35], -4.0\n"
    "v_cvt_pk_bf16_f32 v63, v14, v15\n"
    "v_exp_f32 v0, v0\n"
    "v_mfma_f32_16x16x32_bf16 v[8:11], a[100:103], v[36:39], v[8:11]\n"
    "ds_write_b128 v222, v[60:63] offset:3072\n"
    "v_exp_f32 v1, v1\n"
    "v_mfma_f32_16x16x32_bf16 v[8:11], a[104:107], v[40:43], v[8:11]\n"
    "v_exp_f32 v2, v2\n"
    "v_cvt_pk_bf16_f32 v72, v0, v1\n"
    "v_mfma_f32_16x16x32_bf16 v[8:11], a[108:111], v[44:47], v[8:11]\n"
    "v_exp_f32 v3, v3\n"
    "v_mfma_f32_16x16x32_bf16 v[12:15], a[112:115], v[32:35], -4.0\n"
    "v_cvt_pk_bf16_f32 v73, v2, v3\n"
    "v_exp_f32 v4, v4\n"
    "v_mfma_f32_16x16x32_bf16 v[12:15], a[116:119], v[36:39], v[12:15]\n"
    "v_exp_f32 v5, v5\n"
    "v_mfma_f32_16x16x32_bf16 v[12:15], a[120:123], v[40:43], v[12:15]\n"
    "v_exp_f32 v6, v6\n"
    "v_cvt_pk_bf16_f32 v74, v4, v5\n"
    "v_mfma_f32_16x16x32_bf16 v[12:15], a[124:127], v[44:47], v[12:15]\n"
    "v_exp_f32 v7, v7\n"
    "v_exp_f32 v8, v8\n"
    "v_cvt_pk_bf16_f32 v75, v6, v7\n"
    "v_exp_f32 v9, v9\n"
    "ds_write_b128 v222, v[72:75] offset:6144\n"
    "v_exp_f32 v10, v10\n"
    "v_cvt_pk_bf16_f32 v76, v8, v9\n"
    "v_exp_f32 v11, v11\n"
    "s_nop 0\n"
    "v_cvt_pk_bf16_f32 v77, v10, v11\n"
    "s_waitcnt vmcnt(14) lgkmcnt(0)\n"
    "s_barrier\n"
    "ds_read_b64_tr_b16 a[224:225], v236 offset:0\n"
    "ds_read_b64_tr_b16 a[226:227], v236 offset:4096\n"
    "ds_read_b64_tr_b16 a[232:233], v237 offset:0\n"
    "ds_read_b64_tr_b16 a[234:235], v237 offset:4096\n"
    "ds_read_b64_tr_b16 a[240:241], v238 offset:0\n"
    "ds_read_b64_tr_b16 a[242:243], v238 offset:4096\n"
    "ds_read_b64_tr_b16 a[248:249], v239 offset:0\n"
    "ds_read_b64_tr_b16 a[250:251], v239 offset:4096\n"
    "ds_read_b64_tr_b16 a[228:229], v236 offset:8\n"
    "ds_read_b64_tr_b16 a[230:231], v236 offset:4104\n"
    "ds_read_b64_tr_b16 a[236:237], v237 offset:8\n"
    "ds_read_b64_tr_b16 a[238:239], v237 offset:4104\n"
    "ds_read_b64_tr_b16 a[244:245], v238 offset:8\n"
    "ds_read_b64_tr_b16 a[246:247], v238 offset:4104\n"
    "ds_read_b64_tr_b16 a[252:253], v239 offset:8\n"
    "s_waitcnt lgkmcnt(7)\n"
    "ds_read_b64_tr_b16 a[254:255], v239 offset:4104\n"
    "v_mfma_f32_16x16x32_bf16 v[160:163], a[224:227], v[192:195], v[160:163]\n"
    "v_mfma_f32_16x16x32_bf16 v[160:163], a[232:235], v[112:115], v[160:163]\n"
    "v_mfma_f32_16x16x32_bf16 v[160:163], a[240:243], v[116:119], v[160:163]\n"
    "v_mfma_f32_16x16x32_bf16 v[160:163], a[248:251], v[120:123], v[160:163]\n"
    "s_waitcnt lgkmcnt(6)\n"
    "v_mfma_f32_16x16x32_bf16 v[164:167], a[228:231], v[192:195], v[164:167]\n"
    "s_waitcnt lgkmcnt(4)\n"
    "v_mfma_f32_16x16x32_bf16 v[164:167], a[236:239], v[112:115], v[164:167]\n"
    "s_waitcnt lgkmcnt(2)\n"
    "v_mfma_f32_16x16x32_bf16 v[164:167], a[244:247], v[116:119], v[164:167]\n"
    "s_waitcnt lgkmcnt(0)\n"
    "v_mfma_f32_16x16x32_bf16 v[164:167], a[252:255], v[120:123], v[164:167]\n"
    "v_mfma_f32_16x16x32_bf16 v[168:171], a[224:227], v[196:199], v[168:171]\n"
    "s_waitcnt lgkmcnt(0)\n"
    "s_barrier\n"
    "v_mfma_f32_16x16x32_bf16 v[168:171], a[232:235], v[124:127], v[168:171]\n"
    "ds_write_b32 v226, v200 offset:512\n"
    "v_mfma_f32_16x16x32_bf16 v[168:171], a[240:243], v[128:131], v[168:171]\n"
    "v_mfma_f32_16x16x32_bf16 v[168:171], a[248:251], v[132:135], v[168:171]\n"
    "v_mfma_f32_16x16x32_bf16 v[172:175], a[228:231], v[196:199], v[172:175]\n"
    "v_mul_f32 v160, v160, v216\n"
    "v_mul_f32 v161, v161, v216\n"
    "v_mfma_f32_16x16x32_bf16 v[172:175], a[236:239], v[124:127], v[172:175]\n"
    "v_mul_f32 v162, v162, v216\n"
    "v_mul_f32 v163, v163, v216\n"
    "v_mfma_f32_16x16x32_bf16 v[172:175], a[244:247], v[128:131], v[172:175]\n"
    "v_mul_f32 v164, v164, v216\n"
    "v_mul_f32 v165, v165, v216\n"
    "v_mfma_f32_16x16x32_bf16 v[172:175], a[252:255], v[132:135], v[172:175]\n"
    "v_mul_f32 v166, v166, v216\n"
    "v_mul_f32 v167, v167, v216\n"
    "v_cvt_pk_bf16_f32 v192, v160, v161\n"
    "v_mfma_f32_16x16x32_bf16 v[176:179], a[224:227], v[92:95], v[176:179]\n"
    "v_cvt_pk_bf16_f32 v193, v162, v163\n"
    "v_cvt_pk_bf16_f32 v194, v164, v165\n"
    "v_mfma_f32_16x16x32_bf16 v[176:179], a[232:235], v[136:139], v[176:179]\n"
    "v_cvt_pk_bf16_f32 v195, v166, v167\n"
    "buffer_store_dwordx4 v[192:195], v229, s[52:55], s56 offen offset:0 sc0 sc1\n"
    "v_mfma_f32_16x16x32_bf16 v[176:179], a[240:243], v[140:143], v[176:179]\n"
    "v_mul_f32 v168, v168, v217\n"
    "v_mul_f32 v169, v169, v217\n"
    "v_mfma_f32_16x16x32_bf16 v[176:179], a[248:251], v[144:147], v[176:179]\n"
    "v_mul_f32 v170, v170, v217\n"
    "v_mul_f32 v171, v171, v217\n"
    "v_mfma_f32_16x16x32_bf16 v[180:183], a[228:231], v[92:95], v[180:183]\n"
    "v_mul_f32 v172, v172, v217\n"
    "v_mul_f32 v173, v173, v217\n"
    "v_mfma_f32_16x16x32_bf16 v[180:183], a[236:239], v[136:139], v[180:183]\n"
    "v_mul_f32 v174, v174, v217\n"
    "v_mul_f32 v175, v175, v217\n"
    "v_mfma_f32_16x16x32_bf16 v[180:183], a[244:247], v[140:143], v[180:183]\n"
    "v_cvt_pk_bf16_f32 v196, v168, v169\n"
    "v_cvt_pk_bf16_f32 v197, v170, v171\n"
    "v_mfma_f32_16x16x32_bf16 v[180:183], a[252:255], v[144:147], v[180:183]\n"
    "v_cvt_pk_bf16_f32 v198, v172, v173\n"
    "v_cvt_pk_bf16_f32 v199, v174, v175\n"
    "v_mfma_f32_16x16x32_bf16 v[184:187], a[224:227], v[108:111], v[184:187]\n"
    "buffer_store_dwordx4 v[196:199], v230, s[52:55], s56 offen offset:0 sc0 sc1\n"
    "v_mfma_f32_16x16x32_bf16 v[184:187], a[232:235], v[148:151], v[184:187]\n"
    "v_mfma_f32_16x16x32_bf16 v[184:187], a[240:243], v[152:155], v[184:187]\n"
    "v_mfma_f32_16x16x32_bf16 v[184:187], a[248:251], v[156:159], v[184:187]\n"
    "v_mfma_f32_16x16x32_bf16 v[188:191], a[228:231], v[108:111], v[188:191]\n"
    "v_mfma_f32_16x16x32_bf16 v[188:191], a[236:239], v[148:151], v[188:191]\n"
    "v_mfma_f32_16x16x32_bf16 v[188:191], a[244:247], v[152:155], v[188:191]\n"
    "v_mfma_f32_16x16x32_bf16 v[188:191], a[252:255], v[156:159], v[188:191]\n"
    ".p2align 6\n"
    ".Lgqa_loop%=:\n"
    "v_mfma_f32_16x16x32_bf16 v[0:3], a[0:3], v[16:19], -4.0\n"
    "s_add_u32 s56, s56, 0x2000\n"
    "v_exp_f32 v12, v12\n"
    "v_mfma_f32_16x16x32_bf16 v[0:3], a[4:7], v[20:23], v[0:3]\n"
    "s_add_u32 m0, s58, 0\n"
    "v_exp_f32 v13, v13\n"
    "v_mfma_f32_16x16x32_bf16 v[0:3], a[8:11], v[24:27], v[0:3]\n"
    "ds_read_b128 v[32:35], v220 offset:34816\n"
    "v_cvt_pk_bf16_f32 v78, v12, v13\n"
    "v_mfma_f32_16x16x32_bf16 v[0:3], a[12:15], v[28:31], v[0:3]\n"
    "buffer_load_dwordx4 v228, s[36:39], s56 offen offset:0 lds\n"
    "v_mfma_f32_16x16x32_bf16 v[160:163], a[128:131], v[48:51], 0\n"
    "s_cmp_lg_u64 vcc, 0\n"
    "v_exp_f32 v14, v14\n"
    "v_mfma_f32_16x16x32_bf16 v[160:163], a[160:163], v[52:55], v[160:163]\n"
    "s_addc_u32 s57, s57, s57\n"
    "v_exp_f32 v15, v15\n"
    "v_mfma_f32_16x16x32_bf16 v[160:163], a[192:195], v[56:59], v[160:163]\n"
    "ds_read_b128 v[36:39], v220 offset:35072\n"
    "v_cvt_pk_bf16_f32 v79, v14, v15\n"
    "v_mfma_f32_16x16x32_bf16 v[160:163], a[224:227], v[60:63], v[160:163]\n"
    "ds_write_b128 v222, v[76:79] offset:7168\n"
    "v_mfma_f32_16x16x32_bf16 v[4:7], a[16:19], v[16:19], -4.0\n"
    "v_exp_f32 v0, v0\n"
    "v_mfma_f32_16x16x32_bf16 v[4:7], a[20:23], v[20:23], v[4:7]\n"
    "v_exp_f32 v1, v1\n"
    "v_mfma_f32_16x16x32_bf16 v[4:7], a[24:27], v[24:27], v[4:7]\n"
    "ds_read_b128 v[40:43], v220 offset:35328\n"
    "v_cvt_pk_bf16_f32 v80, v0, v1\n"
    "v_mfma_f32_16x16x32_bf16 v[4:7], a[28:31], v[28:31], v[4:7]\n"
    "v_mfma_f32_16x16x32_bf16 v[164:167], a[132:135], v[48:51], 0\n"
    "v_exp_f32 v2, v2\n"
    "v_mfma_f32_16x16x32_bf16 v[164:167], a[164:167], v[52:55], v[164:167]\n"
    "s_waitcnt vmcnt(15) lgkmcnt(1)\n"
    "v_exp_f32 v3, v3\n"
    "v_mfma_f32_16x16x32_bf16 v[164:167], a[196:199], v[56:59], v[164:167]\n"
    "ds_read_b128 v[44:47], v220 offset:35584\n"
    "v_cvt_pk_bf16_f32 v81, v2, v3\n"
    "v_mfma_f32_16x16x32_bf16 v[164:167], a[228:231], v[60:63], v[164:167]\n"
    "s_barrier\n"
    "v_mfma_f32_16x16x32_bf16 v[8:11], a[32:35], v[16:19], -4.0\n"
    "v_exp_f32 v4, v4\n"
    "v_mfma_f32_16x16x32_bf16 v[8:11], a[36:39], v[20:23], v[8:11]\n"
    "v_exp_f32 v5, v5\n"
    "v_mfma_f32_16x16x32_bf16 v[8:11], a[40:43], v[24:27], v[8:11]\n"
    "ds_read_b128 v[112:115], v223 offset:0\n"
    "v_cvt_pk_bf16_f32 v82, v4, v5\n"
    "v_mfma_f32_16x16x32_bf16 v[8:11], a[44:47], v[28:31], v[8:11]\n"
    "ds_read_b128 v[212:215], v227 offset:512\n"
    "v_add_u32 v220, s59, v221\n"
    "v_mfma_f32_16x16x32_bf16 v[168:171], a[128:131], v[64:67], 0\n"
    "v_exp_f32 v6, v6\n"
    "v_mfma_f32_16x16x32_bf16 v[168:171], a[160:163], v[68:71], v[168:171]\n"
    "v_exp_f32 v7, v7\n"
    "v_mfma_f32_16x16x32_bf16 v[168:171], a[192:195], v[72:75], v[168:171]\n"
    "ds_read_b128 v[116:119], v223 offset:1024\n"
    "v_cvt_pk_bf16_f32 v83, v6, v7\n"
    "v_mfma_f32_16x16x32_bf16 v[168:171], a[224:227], v[76:79], v[168:171]\n"
    "ds_write_b128 v222, v[80:83] offset:8192\n"
    "v_mfma_f32_16x16x32_bf16 v[12:15], a[48:51], v[16:19], -4.0\n"
    "v_exp_f32 v8, v8\n"
    "v_mfma_f32_16x16x32_bf16 v[12:15], a[52:55], v[20:23], v[12:15]\n"
    "v_exp_f32 v9, v9\n"
    "v_mfma_f32_16x16x32_bf16 v[12:15], a[56:59], v[24:27], v[12:15]\n"
    "ds_read_b128 v[120:123], v223 offset:2048\n"
    "v_cvt_pk_bf16_f32 v84, v8, v9\n"
    "v_mfma_f32_16x16x32_bf16 v[12:15], a[60:63], v[28:31], v[12:15]\n"
    "buffer_load_dwordx4 v228, s[36:39], s56 offen offset:1024 lds\n"
    "v_mfma_f32_16x16x32_bf16 v[172:175], a[132:135], v[64:67], 0\n"
    "v_exp_f32 v10, v10\n"
    "v_mfma_f32_16x16x32_bf16 v[172:175], a[164:167], v[68:71], v[172:175]\n"
    "v_exp_f32 v11, v11\n"
    "v_mfma_f32_16x16x32_bf16 v[172:175], a[196:199], v[72:75], v[172:175]\n"
    "ds_read_b128 v[124:127], v223 offset:3072\n"
    "v_cvt_pk_bf16_f32 v85, v10, v11\n"
    "v_mfma_f32_16x16x32_bf16 v[172:175], a[228:231], v[76:79], v[172:175]\n"
    "s_waitcnt lgkmcnt(2)\n"
    "ds_write_b64 v222, v[84:85] offset:9216\n"
    "v_mfma_f32_16x16x32_bf16 v[200:203], v[204:207], v[48:51], 0\n"
    "v_add_f32 v218, v212, v213\n"
    "v_add_f32 v219, v214, v215\n"
    "v_mfma_f32_16x16x32_bf16 v[200:203], v[204:207], v[52:55], v[200:203]\n"
    "ds_read_b128 v[128:131], v224 offset:0\n"
    "v_add_f32 v218, v218, v219\n"
    "v_mfma_f32_16x16x32_bf16 v[200:203], v[204:207], v[56:59], v[200:203]\n"
    "v_rcp_f32 v216, v218\n"
    "v_mfma_f32_16x16x32_bf16 v[200:203], v[204:207], v[60:63], v[200:203]\n"
    "v_mov_b32 v217, v216\n"
    "v_mfma_f32_16x16x32_bf16 v[200:203], v[208:211], v[64:67], v[200:203]\n"
    "ds_read_b128 v[132:135], v224 offset:1024\n"
    "v_mfma_f32_16x16x32_bf16 v[200:203], v[208:211], v[68:71], v[200:203]\n"
    "v_permlane16_swap_b32 v216, v217\n"
    "v_mfma_f32_16x16x32_bf16 v[200:203], v[208:211], v[72:75], v[200:203]\n"
    "v_mul_f32 v176, v176, v216\n"
    "v_mul_f32 v177, v177, v216\n"
    "v_mfma_f32_16x16x32_bf16 v[200:203], v[208:211], v[76:79], v[200:203]\n"
    "v_mul_f32 v178, v178, v216\n"
    "v_mul_f32 v179, v179, v216\n"
    "v_mfma_f32_16x16x32_bf16 v[0:3], a[64:67], v[16:19], -4.0\n"
    "v_exp_f32 v12, v12\n"
    "v_mfma_f32_16x16x32_bf16 v[0:3], a[68:71], v[20:23], v[0:3]\n"
    "v_exp_f32 v13, v13\n"
    "v_mfma_f32_16x16x32_bf16 v[0:3], a[72:75], v[24:27], v[0:3]\n"
    "ds_read_b128 v[136:139], v224 offset:2048\n"
    "v_cvt_pk_bf16_f32 v86, v12, v13\n"
    "v_mfma_f32_16x16x32_bf16 v[0:3], a[76:79], v[28:31], v[0:3]\n"
    "s_waitcnt lgkmcnt(1)\n"
    "ds_write_b32 v226, v200 offset:0\n"
    "v_mfma_f32_16x16x32_bf16 v[160:163], a[136:139], v[112:115], v[160:163]\n"
    "v_exp_f32 v14, v14\n"
    "v_mfma_f32_16x16x32_bf16 v[160:163], a[168:171], v[116:119], v[160:163]\n"
    "v_exp_f32 v15, v15\n"
    "v_mfma_f32_16x16x32_bf16 v[160:163], a[200:203], v[120:123], v[160:163]\n"
    "ds_read_b128 v[140:143], v224 offset:3072\n"
    "v_cvt_pk_bf16_f32 v87, v14, v15\n"
    "v_mfma_f32_16x16x32_bf16 v[160:163], a[232:235], v[124:127], v[160:163]\n"
    "ds_write_b64 v222, v[86:87] offset:9224\n"
    "v_mfma_f32_16x16x32_bf16 v[4:7], a[80:83], v[16:19], -4.0\n"
    "v_exp_f32 v0, v0\n"
    "v_mfma_f32_16x16x32_bf16 v[4:7], a[84:87], v[20:23], v[4:7]\n"
    "v_exp_f32 v1, v1\n"
    "v_mfma_f32_16x16x32_bf16 v[4:7], a[88:91], v[24:27], v[4:7]\n"
    "ds_read_b128 v[144:147], v225 offset:0\n"
    "v_cvt_pk_bf16_f32 v88, v0, v1\n"
    "v_mfma_f32_16x16x32_bf16 v[4:7], a[92:95], v[28:31], v[4:7]\n"
    "v_mul_f32 v180, v180, v216\n"
    "v_mul_f32 v181, v181, v216\n"
    "v_mfma_f32_16x16x32_bf16 v[164:167], a[140:143], v[112:115], v[164:167]\n"
    "v_exp_f32 v2, v2\n"
    "v_mfma_f32_16x16x32_bf16 v[164:167], a[172:175], v[116:119], v[164:167]\n"
    "v_exp_f32 v3, v3\n"
    "v_mfma_f32_16x16x32_bf16 v[164:167], a[204:207], v[120:123], v[164:167]\n"
    "ds_read_b128 v[148:151], v225 offset:1024\n"
    "v_cvt_pk_bf16_f32 v89, v2, v3\n"
    "v_mfma_f32_16x16x32_bf16 v[164:167], a[236:239], v[124:127], v[164:167]\n"
    "ds_write_b64 v222, v[88:89] offset:10240\n"
    "v_mfma_f32_16x16x32_bf16 v[8:11], a[96:99], v[16:19], -4.0\n"
    "v_exp_f32 v4, v4\n"
    "v_mfma_f32_16x16x32_bf16 v[8:11], a[100:103], v[20:23], v[8:11]\n"
    "v_exp_f32 v5, v5\n"
    "v_mfma_f32_16x16x32_bf16 v[8:11], a[104:107], v[24:27], v[8:11]\n"
    "ds_read_b128 v[152:155], v225 offset:2048\n"
    "v_cvt_pk_bf16_f32 v90, v4, v5\n"
    "v_mfma_f32_16x16x32_bf16 v[8:11], a[108:111], v[28:31], v[8:11]\n"
    "s_waitcnt lgkmcnt(3)\n"
    "v_mul_f32 v182, v182, v216\n"
    "v_mul_f32 v183, v183, v216\n"
    "v_mfma_f32_16x16x32_bf16 v[160:163], a[144:147], v[128:131], v[160:163]\n"
    "v_exp_f32 v6, v6\n"
    "v_mfma_f32_16x16x32_bf16 v[160:163], a[176:179], v[132:135], v[160:163]\n"
    "v_exp_f32 v7, v7\n"
    "v_mfma_f32_16x16x32_bf16 v[160:163], a[208:211], v[136:139], v[160:163]\n"
    "ds_read_b128 v[156:159], v225 offset:3072\n"
    "v_cvt_pk_bf16_f32 v91, v6, v7\n"
    "v_mfma_f32_16x16x32_bf16 v[160:163], a[240:243], v[140:143], v[160:163]\n"
    "ds_write_b64 v222, v[90:91] offset:10248\n"
    "v_mfma_f32_16x16x32_bf16 v[12:15], a[112:115], v[16:19], -4.0\n"
    "v_exp_f32 v8, v8\n"
    "v_mfma_f32_16x16x32_bf16 v[12:15], a[116:119], v[20:23], v[12:15]\n"
    "v_exp_f32 v9, v9\n"
    "v_mfma_f32_16x16x32_bf16 v[12:15], a[120:123], v[24:27], v[12:15]\n"
    "ds_read_b128 v[112:115], v223 offset:4096\n"
    "v_cvt_pk_bf16_f32 v92, v8, v9\n"
    "v_mfma_f32_16x16x32_bf16 v[12:15], a[124:127], v[28:31], v[12:15]\n"
    "v_cvt_pk_bf16_f32 v192, v176, v177\n"
    "v_cvt_pk_bf16_f32 v193, v178, v179\n"
    "v_mfma_f32_16x16x32_bf16 v[164:167], a[148:151], v[128:131], v[164:167]\n"
    "v_exp_f32 v10, v10\n"
    "v_mfma_f32_16x16x32_bf16 v[164:167], a[180:183], v[132:135], v[164:167]\n"
    "v_exp_f32 v11, v11\n"
    "v_mfma_f32_16x16x32_bf16 v[164:167], a[212:215], v[136:139], v[164:167]\n"
    "ds_read_b128 v[116:119], v223 offset:5120\n"
    "v_cvt_pk_bf16_f32 v93, v10, v11\n"
    "v_mfma_f32_16x16x32_bf16 v[164:167], a[244:247], v[140:143], v[164:167]\n"
    "ds_write_b64 v222, v[92:93] offset:11264\n"
    "v_mfma_f32_16x16x32_bf16 v[0:3], a[0:3], v[32:35], -4.0\n"
    "v_exp_f32 v12, v12\n"
    "v_mfma_f32_16x16x32_bf16 v[0:3], a[4:7], v[36:39], v[0:3]\n"
    "v_exp_f32 v13, v13\n"
    "v_mfma_f32_16x16x32_bf16 v[0:3], a[8:11], v[40:43], v[0:3]\n"
    "ds_read_b128 v[120:123], v223 offset:6144\n"
    "v_cvt_pk_bf16_f32 v94, v12, v13\n"
    "v_mfma_f32_16x16x32_bf16 v[0:3], a[12:15], v[44:47], v[0:3]\n"
    "s_waitcnt lgkmcnt(3)\n"
    "v_cvt_pk_bf16_f32 v194, v180, v181\n"
    "v_cvt_pk_bf16_f32 v195, v182, v183\n"
    "v_mfma_f32_16x16x32_bf16 v[160:163], a[152:155], v[144:147], v[160:163]\n"
    "v_exp_f32 v14, v14\n"
    "v_mfma_f32_16x16x32_bf16 v[160:163], a[184:187], v[148:151], v[160:163]\n"
    "v_exp_f32 v15, v15\n"
    "v_mfma_f32_16x16x32_bf16 v[160:163], a[216:219], v[152:155], v[160:163]\n"
    "ds_read_b128 v[124:127], v223 offset:7168\n"
    "v_cvt_pk_bf16_f32 v95, v14, v15\n"
    "v_mfma_f32_16x16x32_bf16 v[160:163], a[248:251], v[156:159], v[160:163]\n"
    "ds_write_b64 v222, v[94:95] offset:11272\n"
    "v_mfma_f32_16x16x32_bf16 v[4:7], a[16:19], v[32:35], -4.0\n"
    "v_exp_f32 v0, v0\n"
    "v_mfma_f32_16x16x32_bf16 v[4:7], a[20:23], v[36:39], v[4:7]\n"
    "v_exp_f32 v1, v1\n"
    "v_mfma_f32_16x16x32_bf16 v[4:7], a[24:27], v[40:43], v[4:7]\n"
    "ds_read_b128 v[128:131], v224 offset:4096\n"
    "v_cvt_pk_bf16_f32 v96, v0, v1\n"
    "v_mfma_f32_16x16x32_bf16 v[4:7], a[28:31], v[44:47], v[4:7]\n"
    "buffer_store_dwordx4 v[192:195], v229, s[52:55], s56 offen offset:0 sc0 sc1\n"
    "v_mul_f32 v184, v184, v217\n"
    "v_mul_f32 v185, v185, v217\n"
    "v_mfma_f32_16x16x32_bf16 v[164:167], a[156:159], v[144:147], v[164:167]\n"
    "v_exp_f32 v2, v2\n"
    "v_mfma_f32_16x16x32_bf16 v[164:167], a[188:191], v[148:151], v[164:167]\n"
    "v_exp_f32 v3, v3\n"
    "v_mfma_f32_16x16x32_bf16 v[164:167], a[220:223], v[152:155], v[164:167]\n"
    "ds_read_b128 v[132:135], v224 offset:5120\n"
    "v_cvt_pk_bf16_f32 v97, v2, v3\n"
    "v_mfma_f32_16x16x32_bf16 v[164:167], a[252:255], v[156:159], v[164:167]\n"
    "ds_write_b64 v222, v[96:97] offset:12288\n"
    "v_mfma_f32_16x16x32_bf16 v[8:11], a[32:35], v[32:35], -4.0\n"
    "v_exp_f32 v4, v4\n"
    "v_mfma_f32_16x16x32_bf16 v[8:11], a[36:39], v[36:39], v[8:11]\n"
    "v_exp_f32 v5, v5\n"
    "v_mfma_f32_16x16x32_bf16 v[8:11], a[40:43], v[40:43], v[8:11]\n"
    "ds_read_b128 v[136:139], v224 offset:6144\n"
    "v_cvt_pk_bf16_f32 v98, v4, v5\n"
    "v_mfma_f32_16x16x32_bf16 v[8:11], a[44:47], v[44:47], v[8:11]\n"
    "s_waitcnt lgkmcnt(3)\n"
    "v_mul_f32 v186, v186, v217\n"
    "v_mul_f32 v187, v187, v217\n"
    "v_mfma_f32_16x16x32_bf16 v[168:171], a[136:139], v[112:115], v[168:171]\n"
    "v_exp_f32 v6, v6\n"
    "v_mfma_f32_16x16x32_bf16 v[168:171], a[168:171], v[116:119], v[168:171]\n"
    "v_exp_f32 v7, v7\n"
    "v_mfma_f32_16x16x32_bf16 v[168:171], a[200:203], v[120:123], v[168:171]\n"
    "ds_read_b128 v[140:143], v224 offset:7168\n"
    "v_cvt_pk_bf16_f32 v99, v6, v7\n"
    "v_mfma_f32_16x16x32_bf16 v[168:171], a[232:235], v[124:127], v[168:171]\n"
    "ds_write_b64 v222, v[98:99] offset:12296\n"
    "v_mfma_f32_16x16x32_bf16 v[12:15], a[48:51], v[32:35], -4.0\n"
    "v_exp_f32 v8, v8\n"
    "v_mfma_f32_16x16x32_bf16 v[12:15], a[52:55], v[36:39], v[12:15]\n"
    "v_exp_f32 v9, v9\n"
    "v_mfma_f32_16x16x32_bf16 v[12:15], a[56:59], v[40:43], v[12:15]\n"
    "ds_read_b128 v[144:147], v225 offset:4096\n"
    "v_cvt_pk_bf16_f32 v100, v8, v9\n"
    "v_mfma_f32_16x16x32_bf16 v[12:15], a[60:63], v[44:47], v[12:15]\n"
    "v_mul_f32 v188, v188, v217\n"
    "v_mul_f32 v189, v189, v217\n"
    "v_mfma_f32_16x16x32_bf16 v[172:175], a[140:143], v[112:115], v[172:175]\n"
    "v_exp_f32 v10, v10\n"
    "v_mfma_f32_16x16x32_bf16 v[172:175], a[172:175], v[116:119], v[172:175]\n"
    "v_exp_f32 v11, v11\n"
    "v_mfma_f32_16x16x32_bf16 v[172:175], a[204:207], v[120:123], v[172:175]\n"
    "ds_read_b128 v[148:151], v225 offset:5120\n"
    "v_cvt_pk_bf16_f32 v101, v10, v11\n"
    "v_mfma_f32_16x16x32_bf16 v[172:175], a[236:239], v[124:127], v[172:175]\n"
    "ds_write_b64 v222, v[100:101] offset:13312\n"
    "v_mfma_f32_16x16x32_bf16 v[0:3], a[64:67], v[32:35], -4.0\n"
    "v_exp_f32 v12, v12\n"
    "v_mfma_f32_16x16x32_bf16 v[0:3], a[68:71], v[36:39], v[0:3]\n"
    "v_exp_f32 v13, v13\n"
    "v_mfma_f32_16x16x32_bf16 v[0:3], a[72:75], v[40:43], v[0:3]\n"
    "ds_read_b128 v[152:155], v225 offset:6144\n"
    "v_cvt_pk_bf16_f32 v102, v12, v13\n"
    "v_mfma_f32_16x16x32_bf16 v[0:3], a[76:79], v[44:47], v[0:3]\n"
    "s_waitcnt lgkmcnt(3)\n"
    "v_mul_f32 v190, v190, v217\n"
    "v_mul_f32 v191, v191, v217\n"
    "v_mfma_f32_16x16x32_bf16 v[168:171], a[144:147], v[128:131], v[168:171]\n"
    "v_exp_f32 v14, v14\n"
    "v_mfma_f32_16x16x32_bf16 v[168:171], a[176:179], v[132:135], v[168:171]\n"
    "v_exp_f32 v15, v15\n"
    "v_mfma_f32_16x16x32_bf16 v[168:171], a[208:211], v[136:139], v[168:171]\n"
    "ds_read_b128 v[156:159], v225 offset:7168\n"
    "v_cvt_pk_bf16_f32 v103, v14, v15\n"
    "v_mfma_f32_16x16x32_bf16 v[168:171], a[240:243], v[140:143], v[168:171]\n"
    "ds_write_b64 v222, v[102:103] offset:13320\n"
    "v_mfma_f32_16x16x32_bf16 v[4:7], a[80:83], v[32:35], -4.0\n"
    "v_exp_f32 v0, v0\n"
    "v_mfma_f32_16x16x32_bf16 v[4:7], a[84:87], v[36:39], v[4:7]\n"
    "v_exp_f32 v1, v1\n"
    "v_mfma_f32_16x16x32_bf16 v[4:7], a[88:91], v[40:43], v[4:7]\n"
    "ds_read_b128 v[16:19], v220 offset:0\n"
    "v_cvt_pk_bf16_f32 v104, v0, v1\n"
    "v_mfma_f32_16x16x32_bf16 v[4:7], a[92:95], v[44:47], v[4:7]\n"
    "v_cvt_pk_bf16_f32 v196, v184, v185\n"
    "v_cvt_pk_bf16_f32 v197, v186, v187\n"
    "v_mfma_f32_16x16x32_bf16 v[172:175], a[148:151], v[128:131], v[172:175]\n"
    "v_exp_f32 v2, v2\n"
    "v_mfma_f32_16x16x32_bf16 v[172:175], a[180:183], v[132:135], v[172:175]\n"
    "v_exp_f32 v3, v3\n"
    "v_mfma_f32_16x16x32_bf16 v[172:175], a[212:215], v[136:139], v[172:175]\n"
    "ds_read_b128 v[20:23], v220 offset:256\n"
    "v_cvt_pk_bf16_f32 v105, v2, v3\n"
    "v_mfma_f32_16x16x32_bf16 v[172:175], a[244:247], v[140:143], v[172:175]\n"
    "ds_write_b64 v222, v[104:105] offset:14336\n"
    "v_mfma_f32_16x16x32_bf16 v[8:11], a[96:99], v[32:35], -4.0\n"
    "v_exp_f32 v4, v4\n"
    "v_mfma_f32_16x16x32_bf16 v[8:11], a[100:103], v[36:39], v[8:11]\n"
    "v_exp_f32 v5, v5\n"
    "v_mfma_f32_16x16x32_bf16 v[8:11], a[104:107], v[40:43], v[8:11]\n"
    "ds_read_b128 v[24:27], v220 offset:512\n"
    "v_cvt_pk_bf16_f32 v106, v4, v5\n"
    "v_mfma_f32_16x16x32_bf16 v[8:11], a[108:111], v[44:47], v[8:11]\n"
    "s_waitcnt lgkmcnt(3)\n"
    "v_cvt_pk_bf16_f32 v198, v188, v189\n"
    "v_cvt_pk_bf16_f32 v199, v190, v191\n"
    "v_mfma_f32_16x16x32_bf16 v[168:171], a[152:155], v[144:147], v[168:171]\n"
    "v_exp_f32 v6, v6\n"
    "v_mfma_f32_16x16x32_bf16 v[168:171], a[184:187], v[148:151], v[168:171]\n"
    "v_exp_f32 v7, v7\n"
    "v_mfma_f32_16x16x32_bf16 v[168:171], a[216:219], v[152:155], v[168:171]\n"
    "ds_read_b128 v[28:31], v220 offset:768\n"
    "v_cvt_pk_bf16_f32 v107, v6, v7\n"
    "v_mfma_f32_16x16x32_bf16 v[168:171], a[248:251], v[156:159], v[168:171]\n"
    "ds_write_b64 v222, v[106:107] offset:14344\n"
    "v_mfma_f32_16x16x32_bf16 v[12:15], a[112:115], v[32:35], -4.0\n"
    "v_exp_f32 v8, v8\n"
    "v_mfma_f32_16x16x32_bf16 v[12:15], a[116:119], v[36:39], v[12:15]\n"
    "v_exp_f32 v9, v9\n"
    "v_mfma_f32_16x16x32_bf16 v[12:15], a[120:123], v[40:43], v[12:15]\n"
    "v_cvt_pk_bf16_f32 v108, v8, v9\n"
    "v_subrev_u32 v219, s63, v218\n"
    "v_mfma_f32_16x16x32_bf16 v[12:15], a[124:127], v[44:47], v[12:15]\n"
    "buffer_store_dwordx4 v[196:199], v230, s[52:55], s56 offen offset:0 sc0 sc1\n"
    "v_mfma_f32_16x16x32_bf16 v[172:175], a[156:159], v[144:147], v[172:175]\n"
    "v_exp_f32 v10, v10\n"
    "v_mfma_f32_16x16x32_bf16 v[172:175], a[188:191], v[148:151], v[172:175]\n"
    "v_exp_f32 v11, v11\n"
    "v_mfma_f32_16x16x32_bf16 v[172:175], a[220:223], v[152:155], v[172:175]\n"
    "v_cvt_pk_bf16_f32 v109, v10, v11\n"
    "v_cmp_le_u32 vcc, s64, v219\n"
    "v_mfma_f32_16x16x32_bf16 v[172:175], a[252:255], v[156:159], v[172:175]\n"
    "s_waitcnt lgkmcnt(0)\n"
    "v_mfma_f32_16x16x32_bf16 v[0:3], a[0:3], v[16:19], -4.0\n"
    "s_add_u32 s56, s56, 0x2000\n"
    "v_exp_f32 v12, v12\n"
    "v_mfma_f32_16x16x32_bf16 v[0:3], a[4:7], v[20:23], v[0:3]\n"
    "s_add_u32 m0, s58, 2048\n"
    "v_exp_f32 v13, v13\n"
    "v_mfma_f32_16x16x32_bf16 v[0:3], a[8:11], v[24:27], v[0:3]\n"
    "ds_read_b128 v[32:35], v220 offset:32768\n"
    "v_cvt_pk_bf16_f32 v110, v12, v13\n"
    "v_mfma_f32_16x16x32_bf16 v[0:3], a[12:15], v[28:31], v[0:3]\n"
    "buffer_load_dwordx4 v228, s[36:39], s56 offen offset:0 lds\n"
    "v_mfma_f32_16x16x32_bf16 v[176:179], a[128:131], v[80:83], 0\n"
    "s_cmp_lg_u64 vcc, 0\n"
    "v_exp_f32 v14, v14\n"
    "v_mfma_f32_16x16x32_bf16 v[176:179], a[160:163], v[84:87], v[176:179]\n"
    "s_addc_u32 s57, s57, s57\n"
    "v_exp_f32 v15, v15\n"
    "v_mfma_f32_16x16x32_bf16 v[176:179], a[192:195], v[88:91], v[176:179]\n"
    "ds_read_b128 v[36:39], v220 offset:33024\n"
    "v_cvt_pk_bf16_f32 v111, v14, v15\n"
    "v_mfma_f32_16x16x32_bf16 v[176:179], a[224:227], v[92:95], v[176:179]\n"
    "ds_write_b128 v222, v[108:111] offset:15360\n"
    "v_mfma_f32_16x16x32_bf16 v[4:7], a[16:19], v[16:19], -4.0\n"
    "v_exp_f32 v0, v0\n"
    "v_mfma_f32_16x16x32_bf16 v[4:7], a[20:23], v[20:23], v[4:7]\n"
    "v_exp_f32 v1, v1\n"
    "v_mfma_f32_16x16x32_bf16 v[4:7], a[24:27], v[24:27], v[4:7]\n"
    "ds_read_b128 v[40:43], v220 offset:33280\n"
    "v_cvt_pk_bf16_f32 v48, v0, v1\n"
    "v_mfma_f32_16x16x32_bf16 v[4:7], a[28:31], v[28:31], v[4:7]\n"
    "v_mfma_f32_16x16x32_bf16 v[180:183], a[132:135], v[80:83], 0\n"
    "v_exp_f32 v2, v2\n"
    "v_mfma_f32_16x16x32_bf16 v[180:183], a[164:167], v[84:87], v[180:183]\n"
    "s_waitcnt vmcnt(15) lgkmcnt(1)\n"
    "v_exp_f32 v3, v3\n"
    "v_mfma_f32_16x16x32_bf16 v[180:183], a[196:199], v[88:91], v[180:183]\n"
    "ds_read_b128 v[44:47], v220 offset:33536\n"
    "v_cvt_pk_bf16_f32 v49, v2, v3\n"
    "v_mfma_f32_16x16x32_bf16 v[180:183], a[228:231], v[92:95], v[180:183]\n"
    "s_barrier\n"
    "v_mfma_f32_16x16x32_bf16 v[8:11], a[32:35], v[16:19], -4.0\n"
    "v_exp_f32 v4, v4\n"
    "v_mfma_f32_16x16x32_bf16 v[8:11], a[36:39], v[20:23], v[8:11]\n"
    "v_exp_f32 v5, v5\n"
    "v_mfma_f32_16x16x32_bf16 v[8:11], a[40:43], v[24:27], v[8:11]\n"
    "ds_read_b128 v[112:115], v223 offset:8192\n"
    "v_cvt_pk_bf16_f32 v50, v4, v5\n"
    "v_mfma_f32_16x16x32_bf16 v[8:11], a[44:47], v[28:31], v[8:11]\n"
    "ds_read_b128 v[212:215], v227 offset:0\n"
    "v_mfma_f32_16x16x32_bf16 v[184:187], a[128:131], v[96:99], 0\n"
    "v_exp_f32 v6, v6\n"
    "v_mfma_f32_16x16x32_bf16 v[184:187], a[160:163], v[100:103], v[184:187]\n"
    "v_exp_f32 v7, v7\n"
    "v_mfma_f32_16x16x32_bf16 v[184:187], a[192:195], v[104:107], v[184:187]\n"
    "ds_read_b128 v[116:119], v223 offset:9216\n"
    "v_cvt_pk_bf16_f32 v51, v6, v7\n"
    "v_mfma_f32_16x16x32_bf16 v[184:187], a[224:227], v[108:111], v[184:187]\n"
    "ds_write_b128 v222, v[48:51] offset:0\n"
    "v_mfma_f32_16x16x32_bf16 v[12:15], a[48:51], v[16:19], -4.0\n"
    "v_exp_f32 v8, v8\n"
    "v_mfma_f32_16x16x32_bf16 v[12:15], a[52:55], v[20:23], v[12:15]\n"
    "v_exp_f32 v9, v9\n"
    "v_mfma_f32_16x16x32_bf16 v[12:15], a[56:59], v[24:27], v[12:15]\n"
    "ds_read_b128 v[120:123], v223 offset:10240\n"
    "v_cvt_pk_bf16_f32 v52, v8, v9\n"
    "v_mfma_f32_16x16x32_bf16 v[12:15], a[60:63], v[28:31], v[12:15]\n"
    "buffer_load_dwordx4 v228, s[36:39], s56 offen offset:1024 lds\n"
    "v_mfma_f32_16x16x32_bf16 v[188:191], a[132:135], v[96:99], 0\n"
    "v_exp_f32 v10, v10\n"
    "v_mfma_f32_16x16x32_bf16 v[188:191], a[164:167], v[100:103], v[188:191]\n"
    "v_exp_f32 v11, v11\n"
    "v_mfma_f32_16x16x32_bf16 v[188:191], a[196:199], v[104:107], v[188:191]\n"
    "ds_read_b128 v[124:127], v223 offset:11264\n"
    "v_cvt_pk_bf16_f32 v53, v10, v11\n"
    "v_mfma_f32_16x16x32_bf16 v[188:191], a[228:231], v[108:111], v[188:191]\n"
    "s_waitcnt lgkmcnt(2)\n"
    "ds_write_b64 v222, v[52:53] offset:1024\n"
    "v_mfma_f32_16x16x32_bf16 v[200:203], v[204:207], v[80:83], 0\n"
    "v_add_f32 v218, v212, v213\n"
    "v_add_f32 v219, v214, v215\n"
    "v_mfma_f32_16x16x32_bf16 v[200:203], v[204:207], v[84:87], v[200:203]\n"
    "ds_read_b128 v[128:131], v224 offset:8192\n"
    "v_add_f32 v218, v218, v219\n"
    "v_mfma_f32_16x16x32_bf16 v[200:203], v[204:207], v[88:91], v[200:203]\n"
    "v_rcp_f32 v216, v218\n"
    "v_mfma_f32_16x16x32_bf16 v[200:203], v[204:207], v[92:95], v[200:203]\n"
    "v_mov_b32 v217, v216\n"
    "v_mfma_f32_16x16x32_bf16 v[200:203], v[208:211], v[96:99], v[200:203]\n"
    "ds_read_b128 v[132:135], v224 offset:9216\n"
    "v_mfma_f32_16x16x32_bf16 v[200:203], v[208:211], v[100:103], v[200:203]\n"
    "v_permlane16_swap_b32 v216, v217\n"
    "v_mfma_f32_16x16x32_bf16 v[200:203], v[208:211], v[104:107], v[200:203]\n"
    "v_mul_f32 v160, v160, v216\n"
    "v_mul_f32 v161, v161, v216\n"
    "v_mfma_f32_16x16x32_bf16 v[200:203], v[208:211], v[108:111], v[200:203]\n"
    "v_mul_f32 v162, v162, v216\n"
    "v_mul_f32 v163, v163, v216\n"
    "v_mfma_f32_16x16x32_bf16 v[0:3], a[64:67], v[16:19], -4.0\n"
    "v_exp_f32 v12, v12\n"
    "v_mfma_f32_16x16x32_bf16 v[0:3], a[68:71], v[20:23], v[0:3]\n"
    "v_exp_f32 v13, v13\n"
    "v_mfma_f32_16x16x32_bf16 v[0:3], a[72:75], v[24:27], v[0:3]\n"
    "ds_read_b128 v[136:139], v224 offset:10240\n"
    "v_cvt_pk_bf16_f32 v54, v12, v13\n"
    "v_mfma_f32_16x16x32_bf16 v[0:3], a[76:79], v[28:31], v[0:3]\n"
    "s_waitcnt lgkmcnt(1)\n"
    "ds_write_b32 v226, v200 offset:512\n"
    "v_mfma_f32_16x16x32_bf16 v[176:179], a[136:139], v[112:115], v[176:179]\n"
    "v_exp_f32 v14, v14\n"
    "v_mfma_f32_16x16x32_bf16 v[176:179], a[168:171], v[116:119], v[176:179]\n"
    "v_exp_f32 v15, v15\n"
    "v_mfma_f32_16x16x32_bf16 v[176:179], a[200:203], v[120:123], v[176:179]\n"
    "ds_read_b128 v[140:143], v224 offset:11264\n"
    "v_cvt_pk_bf16_f32 v55, v14, v15\n"
    "v_mfma_f32_16x16x32_bf16 v[176:179], a[232:235], v[124:127], v[176:179]\n"
    "ds_write_b64 v222, v[54:55] offset:1032\n"
    "v_mfma_f32_16x16x32_bf16 v[4:7], a[80:83], v[16:19], -4.0\n"
    "v_exp_f32 v0, v0\n"
    "v_mfma_f32_16x16x32_bf16 v[4:7], a[84:87], v[20:23], v[4:7]\n"
    "v_exp_f32 v1, v1\n"
    "v_mfma_f32_16x16x32_bf16 v[4:7], a[88:91], v[24:27], v[4:7]\n"
    "ds_read_b128 v[144:147], v225 offset:8192\n"
    "v_cvt_pk_bf16_f32 v56, v0, v1\n"
    "v_mfma_f32_16x16x32_bf16 v[4:7], a[92:95], v[28:31], v[4:7]\n"
    "v_mul_f32 v164, v164, v216\n"
    "v_mul_f32 v165, v165, v216\n"
    "v_mfma_f32_16x16x32_bf16 v[180:183], a[140:143], v[112:115], v[180:183]\n"
    "v_exp_f32 v2, v2\n"
    "v_mfma_f32_16x16x32_bf16 v[180:183], a[172:175], v[116:119], v[180:183]\n"
    "v_exp_f32 v3, v3\n"
    "v_mfma_f32_16x16x32_bf16 v[180:183], a[204:207], v[120:123], v[180:183]\n"
    "ds_read_b128 v[148:151], v225 offset:9216\n"
    "v_cvt_pk_bf16_f32 v57, v2, v3\n"
    "v_mfma_f32_16x16x32_bf16 v[180:183], a[236:239], v[124:127], v[180:183]\n"
    "ds_write_b64 v222, v[56:57] offset:2048\n"
    "v_mfma_f32_16x16x32_bf16 v[8:11], a[96:99], v[16:19], -4.0\n"
    "v_exp_f32 v4, v4\n"
    "v_mfma_f32_16x16x32_bf16 v[8:11], a[100:103], v[20:23], v[8:11]\n"
    "v_exp_f32 v5, v5\n"
    "v_mfma_f32_16x16x32_bf16 v[8:11], a[104:107], v[24:27], v[8:11]\n"
    "ds_read_b128 v[152:155], v225 offset:10240\n"
    "v_cvt_pk_bf16_f32 v58, v4, v5\n"
    "v_mfma_f32_16x16x32_bf16 v[8:11], a[108:111], v[28:31], v[8:11]\n"
    "s_waitcnt lgkmcnt(3)\n"
    "v_mul_f32 v166, v166, v216\n"
    "v_mul_f32 v167, v167, v216\n"
    "v_mfma_f32_16x16x32_bf16 v[176:179], a[144:147], v[128:131], v[176:179]\n"
    "v_exp_f32 v6, v6\n"
    "v_mfma_f32_16x16x32_bf16 v[176:179], a[176:179], v[132:135], v[176:179]\n"
    "v_exp_f32 v7, v7\n"
    "v_mfma_f32_16x16x32_bf16 v[176:179], a[208:211], v[136:139], v[176:179]\n"
    "ds_read_b128 v[156:159], v225 offset:11264\n"
    "v_cvt_pk_bf16_f32 v59, v6, v7\n"
    "v_mfma_f32_16x16x32_bf16 v[176:179], a[240:243], v[140:143], v[176:179]\n"
    "ds_write_b64 v222, v[58:59] offset:2056\n"
    "v_mfma_f32_16x16x32_bf16 v[12:15], a[112:115], v[16:19], -4.0\n"
    "v_exp_f32 v8, v8\n"
    "v_mfma_f32_16x16x32_bf16 v[12:15], a[116:119], v[20:23], v[12:15]\n"
    "v_exp_f32 v9, v9\n"
    "v_mfma_f32_16x16x32_bf16 v[12:15], a[120:123], v[24:27], v[12:15]\n"
    "ds_read_b128 v[112:115], v223 offset:12288\n"
    "v_cvt_pk_bf16_f32 v60, v8, v9\n"
    "v_mfma_f32_16x16x32_bf16 v[12:15], a[124:127], v[28:31], v[12:15]\n"
    "v_cvt_pk_bf16_f32 v192, v160, v161\n"
    "v_cvt_pk_bf16_f32 v193, v162, v163\n"
    "v_mfma_f32_16x16x32_bf16 v[180:183], a[148:151], v[128:131], v[180:183]\n"
    "v_exp_f32 v10, v10\n"
    "v_mfma_f32_16x16x32_bf16 v[180:183], a[180:183], v[132:135], v[180:183]\n"
    "v_exp_f32 v11, v11\n"
    "v_mfma_f32_16x16x32_bf16 v[180:183], a[212:215], v[136:139], v[180:183]\n"
    "ds_read_b128 v[116:119], v223 offset:13312\n"
    "v_cvt_pk_bf16_f32 v61, v10, v11\n"
    "v_mfma_f32_16x16x32_bf16 v[180:183], a[244:247], v[140:143], v[180:183]\n"
    "ds_write_b64 v222, v[60:61] offset:3072\n"
    "v_mfma_f32_16x16x32_bf16 v[0:3], a[0:3], v[32:35], -4.0\n"
    "v_exp_f32 v12, v12\n"
    "v_mfma_f32_16x16x32_bf16 v[0:3], a[4:7], v[36:39], v[0:3]\n"
    "v_exp_f32 v13, v13\n"
    "v_mfma_f32_16x16x32_bf16 v[0:3], a[8:11], v[40:43], v[0:3]\n"
    "ds_read_b128 v[120:123], v223 offset:14336\n"
    "v_cvt_pk_bf16_f32 v62, v12, v13\n"
    "v_mfma_f32_16x16x32_bf16 v[0:3], a[12:15], v[44:47], v[0:3]\n"
    "s_waitcnt lgkmcnt(3)\n"
    "v_cvt_pk_bf16_f32 v194, v164, v165\n"
    "v_cvt_pk_bf16_f32 v195, v166, v167\n"
    "v_mfma_f32_16x16x32_bf16 v[176:179], a[152:155], v[144:147], v[176:179]\n"
    "v_exp_f32 v14, v14\n"
    "v_mfma_f32_16x16x32_bf16 v[176:179], a[184:187], v[148:151], v[176:179]\n"
    "v_exp_f32 v15, v15\n"
    "v_mfma_f32_16x16x32_bf16 v[176:179], a[216:219], v[152:155], v[176:179]\n"
    "ds_read_b128 v[124:127], v223 offset:15360\n"
    "v_cvt_pk_bf16_f32 v63, v14, v15\n"
    "v_mfma_f32_16x16x32_bf16 v[176:179], a[248:251], v[156:159], v[176:179]\n"
    "ds_write_b64 v222, v[62:63] offset:3080\n"
    "v_mfma_f32_16x16x32_bf16 v[4:7], a[16:19], v[32:35], -4.0\n"
    "v_exp_f32 v0, v0\n"
    "v_mfma_f32_16x16x32_bf16 v[4:7], a[20:23], v[36:39], v[4:7]\n"
    "v_exp_f32 v1, v1\n"
    "v_mfma_f32_16x16x32_bf16 v[4:7], a[24:27], v[40:43], v[4:7]\n"
    "ds_read_b128 v[128:131], v224 offset:12288\n"
    "v_cvt_pk_bf16_f32 v64, v0, v1\n"
    "v_mfma_f32_16x16x32_bf16 v[4:7], a[28:31], v[44:47], v[4:7]\n"
    "buffer_store_dwordx4 v[192:195], v229, s[52:55], s56 offen offset:0 sc0 sc1\n"
    "v_mul_f32 v168, v168, v217\n"
    "v_mul_f32 v169, v169, v217\n"
    "v_mfma_f32_16x16x32_bf16 v[180:183], a[156:159], v[144:147], v[180:183]\n"
    "v_exp_f32 v2, v2\n"
    "v_mfma_f32_16x16x32_bf16 v[180:183], a[188:191], v[148:151], v[180:183]\n"
    "v_exp_f32 v3, v3\n"
    "v_mfma_f32_16x16x32_bf16 v[180:183], a[220:223], v[152:155], v[180:183]\n"
    "ds_read_b128 v[132:135], v224 offset:13312\n"
    "v_cvt_pk_bf16_f32 v65, v2, v3\n"
    "v_mfma_f32_16x16x32_bf16 v[180:183], a[252:255], v[156:159], v[180:183]\n"
    "ds_write_b64 v222, v[64:65] offset:4096\n"
    "v_mfma_f32_16x16x32_bf16 v[8:11], a[32:35], v[32:35], -4.0\n"
    "v_exp_f32 v4, v4\n"
    "v_mfma_f32_16x16x32_bf16 v[8:11], a[36:39], v[36:39], v[8:11]\n"
    "v_exp_f32 v5, v5\n"
    "v_mfma_f32_16x16x32_bf16 v[8:11], a[40:43], v[40:43], v[8:11]\n"
    "ds_read_b128 v[136:139], v224 offset:14336\n"
    "v_cvt_pk_bf16_f32 v66, v4, v5\n"
    "v_mfma_f32_16x16x32_bf16 v[8:11], a[44:47], v[44:47], v[8:11]\n"
    "s_waitcnt lgkmcnt(3)\n"
    "v_mul_f32 v170, v170, v217\n"
    "v_mul_f32 v171, v171, v217\n"
    "v_mfma_f32_16x16x32_bf16 v[184:187], a[136:139], v[112:115], v[184:187]\n"
    "v_exp_f32 v6, v6\n"
    "v_mfma_f32_16x16x32_bf16 v[184:187], a[168:171], v[116:119], v[184:187]\n"
    "v_exp_f32 v7, v7\n"
    "v_mfma_f32_16x16x32_bf16 v[184:187], a[200:203], v[120:123], v[184:187]\n"
    "ds_read_b128 v[140:143], v224 offset:15360\n"
    "v_cvt_pk_bf16_f32 v67, v6, v7\n"
    "v_mfma_f32_16x16x32_bf16 v[184:187], a[232:235], v[124:127], v[184:187]\n"
    "ds_write_b64 v222, v[66:67] offset:4104\n"
    "v_mfma_f32_16x16x32_bf16 v[12:15], a[48:51], v[32:35], -4.0\n"
    "v_exp_f32 v8, v8\n"
    "v_mfma_f32_16x16x32_bf16 v[12:15], a[52:55], v[36:39], v[12:15]\n"
    "v_exp_f32 v9, v9\n"
    "v_mfma_f32_16x16x32_bf16 v[12:15], a[56:59], v[40:43], v[12:15]\n"
    "ds_read_b128 v[144:147], v225 offset:12288\n"
    "v_cvt_pk_bf16_f32 v68, v8, v9\n"
    "v_mfma_f32_16x16x32_bf16 v[12:15], a[60:63], v[44:47], v[12:15]\n"
    "v_mul_f32 v172, v172, v217\n"
    "v_mul_f32 v173, v173, v217\n"
    "v_mfma_f32_16x16x32_bf16 v[188:191], a[140:143], v[112:115], v[188:191]\n"
    "v_exp_f32 v10, v10\n"
    "v_mfma_f32_16x16x32_bf16 v[188:191], a[172:175], v[116:119], v[188:191]\n"
    "v_exp_f32 v11, v11\n"
    "v_mfma_f32_16x16x32_bf16 v[188:191], a[204:207], v[120:123], v[188:191]\n"
    "ds_read_b128 v[148:151], v225 offset:13312\n"
    "v_cvt_pk_bf16_f32 v69, v10, v11\n"
    "v_mfma_f32_16x16x32_bf16 v[188:191], a[236:239], v[124:127], v[188:191]\n"
    "ds_write_b64 v222, v[68:69] offset:5120\n"
    "v_mfma_f32_16x16x32_bf16 v[0:3], a[64:67], v[32:35], -4.0\n"
    "v_exp_f32 v12, v12\n"
    "v_mfma_f32_16x16x32_bf16 v[0:3], a[68:71], v[36:39], v[0:3]\n"
    "v_exp_f32 v13, v13\n"
    "v_mfma_f32_16x16x32_bf16 v[0:3], a[72:75], v[40:43], v[0:3]\n"
    "ds_read_b128 v[152:155], v225 offset:14336\n"
    "v_cvt_pk_bf16_f32 v70, v12, v13\n"
    "v_mfma_f32_16x16x32_bf16 v[0:3], a[76:79], v[44:47], v[0:3]\n"
    "s_waitcnt lgkmcnt(3)\n"
    "v_mul_f32 v174, v174, v217\n"
    "v_mul_f32 v175, v175, v217\n"
    "v_mfma_f32_16x16x32_bf16 v[184:187], a[144:147], v[128:131], v[184:187]\n"
    "v_exp_f32 v14, v14\n"
    "v_mfma_f32_16x16x32_bf16 v[184:187], a[176:179], v[132:135], v[184:187]\n"
    "v_exp_f32 v15, v15\n"
    "v_mfma_f32_16x16x32_bf16 v[184:187], a[208:211], v[136:139], v[184:187]\n"
    "ds_read_b128 v[156:159], v225 offset:15360\n"
    "v_cvt_pk_bf16_f32 v71, v14, v15\n"
    "v_mfma_f32_16x16x32_bf16 v[184:187], a[240:243], v[140:143], v[184:187]\n"
    "ds_write_b64 v222, v[70:71] offset:5128\n"
    "v_mfma_f32_16x16x32_bf16 v[4:7], a[80:83], v[32:35], -4.0\n"
    "v_exp_f32 v0, v0\n"
    "v_mfma_f32_16x16x32_bf16 v[4:7], a[84:87], v[36:39], v[4:7]\n"
    "v_exp_f32 v1, v1\n"
    "v_mfma_f32_16x16x32_bf16 v[4:7], a[88:91], v[40:43], v[4:7]\n"
    "ds_read_b128 v[16:19], v220 offset:2048\n"
    "v_cvt_pk_bf16_f32 v72, v0, v1\n"
    "v_mfma_f32_16x16x32_bf16 v[4:7], a[92:95], v[44:47], v[4:7]\n"
    "v_cvt_pk_bf16_f32 v196, v168, v169\n"
    "v_cvt_pk_bf16_f32 v197, v170, v171\n"
    "v_mfma_f32_16x16x32_bf16 v[188:191], a[148:151], v[128:131], v[188:191]\n"
    "v_exp_f32 v2, v2\n"
    "v_mfma_f32_16x16x32_bf16 v[188:191], a[180:183], v[132:135], v[188:191]\n"
    "v_exp_f32 v3, v3\n"
    "v_mfma_f32_16x16x32_bf16 v[188:191], a[212:215], v[136:139], v[188:191]\n"
    "ds_read_b128 v[20:23], v220 offset:2304\n"
    "v_cvt_pk_bf16_f32 v73, v2, v3\n"
    "v_mfma_f32_16x16x32_bf16 v[188:191], a[244:247], v[140:143], v[188:191]\n"
    "ds_write_b64 v222, v[72:73] offset:6144\n"
    "v_mfma_f32_16x16x32_bf16 v[8:11], a[96:99], v[32:35], -4.0\n"
    "s_add_u32 s59, s59, 0x1000\n"
    "v_exp_f32 v4, v4\n"
    "v_mfma_f32_16x16x32_bf16 v[8:11], a[100:103], v[36:39], v[8:11]\n"
    "s_and_b32 s59, s59, 0x3fff\n"
    "v_exp_f32 v5, v5\n"
    "v_mfma_f32_16x16x32_bf16 v[8:11], a[104:107], v[40:43], v[8:11]\n"
    "ds_read_b128 v[24:27], v220 offset:2560\n"
    "v_cvt_pk_bf16_f32 v74, v4, v5\n"
    "v_mfma_f32_16x16x32_bf16 v[8:11], a[108:111], v[44:47], v[8:11]\n"
    "s_waitcnt lgkmcnt(3)\n"
    "v_cvt_pk_bf16_f32 v198, v172, v173\n"
    "v_cvt_pk_bf16_f32 v199, v174, v175\n"
    "v_mfma_f32_16x16x32_bf16 v[184:187], a[152:155], v[144:147], v[184:187]\n"
    "s_xor_b32 s58, s59, s66\n"
    "v_exp_f32 v6, v6\n"
    "v_mfma_f32_16x16x32_bf16 v[184:187], a[184:187], v[148:151], v[184:187]\n"
    "v_exp_f32 v7, v7\n"
    "v_mfma_f32_16x16x32_bf16 v[184:187], a[216:219], v[152:155], v[184:187]\n"
    "ds_read_b128 v[28:31], v220 offset:2816\n"
    "v_cvt_pk_bf16_f32 v75, v6, v7\n"
    "v_mfma_f32_16x16x32_bf16 v[184:187], a[248:251], v[156:159], v[184:187]\n"
    "ds_write_b64 v222, v[74:75] offset:6152\n"
    "v_mfma_f32_16x16x32_bf16 v[12:15], a[112:115], v[32:35], -4.0\n"
    "v_exp_f32 v8, v8\n"
    "v_mfma_f32_16x16x32_bf16 v[12:15], a[116:119], v[36:39], v[12:15]\n"
    "v_exp_f32 v9, v9\n"
    "v_mfma_f32_16x16x32_bf16 v[12:15], a[120:123], v[40:43], v[12:15]\n"
    "v_cvt_pk_bf16_f32 v76, v8, v9\n"
    "v_subrev_u32 v219, s63, v218\n"
    "v_mfma_f32_16x16x32_bf16 v[12:15], a[124:127], v[44:47], v[12:15]\n"
    "buffer_store_dwordx4 v[196:199], v230, s[52:55], s56 offen offset:0 sc0 sc1\n"
    "v_mfma_f32_16x16x32_bf16 v[188:191], a[156:159], v[144:147], v[188:191]\n"
    "v_exp_f32 v10, v10\n"
    "v_mfma_f32_16x16x32_bf16 v[188:191], a[188:191], v[148:151], v[188:191]\n"
    "s_cmp_lt_u32 s56, 245760\n"
    "v_exp_f32 v11, v11\n"
    "v_mfma_f32_16x16x32_bf16 v[188:191], a[220:223], v[152:155], v[188:191]\n"
    "s_waitcnt lgkmcnt(0)\n"
    "v_cvt_pk_bf16_f32 v77, v10, v11\n"
    "v_cmp_le_u32 vcc, s64, v219\n"
    "v_mfma_f32_16x16x32_bf16 v[188:191], a[252:255], v[156:159], v[188:191]\n"
    "s_cbranch_scc1 .Lgqa_loop%=\n"
    "v_mfma_f32_16x16x32_bf16 v[160:163], a[128:131], v[48:51], 0\n"
    "s_cmp_lg_u64 vcc, 0\n"
    "v_exp_f32 v12, v12\n"
    "v_mfma_f32_16x16x32_bf16 v[160:163], a[160:163], v[52:55], v[160:163]\n"
    "s_addc_u32 s57, s57, s57\n"
    "v_exp_f32 v13, v13\n"
    "v_mfma_f32_16x16x32_bf16 v[160:163], a[192:195], v[56:59], v[160:163]\n"
    "v_exp_f32 v14, v14\n"
    "v_mfma_f32_16x16x32_bf16 v[160:163], a[224:227], v[60:63], v[160:163]\n"
    "v_exp_f32 v15, v15\n"
    "v_cvt_pk_bf16_f32 v78, v12, v13\n"
    "v_mfma_f32_16x16x32_bf16 v[164:167], a[132:135], v[48:51], 0\n"
    "s_add_u32 s56, s56, 0x2000\n"
    "v_cvt_pk_bf16_f32 v79, v14, v15\n"
    "v_mfma_f32_16x16x32_bf16 v[164:167], a[164:167], v[52:55], v[164:167]\n"
    "ds_write_b128 v222, v[76:79] offset:7168\n"
    "v_mfma_f32_16x16x32_bf16 v[164:167], a[196:199], v[56:59], v[164:167]\n"
    "v_mfma_f32_16x16x32_bf16 v[164:167], a[228:231], v[60:63], v[164:167]\n"
    "v_mfma_f32_16x16x32_bf16 v[200:203], v[204:207], v[48:51], 0\n"
    "v_mfma_f32_16x16x32_bf16 v[200:203], v[204:207], v[52:55], v[200:203]\n"
    "s_waitcnt lgkmcnt(0)\n"
    "s_barrier\n"
    "v_mfma_f32_16x16x32_bf16 v[200:203], v[204:207], v[56:59], v[200:203]\n"
    "ds_read_b128 v[212:215], v227 offset:512\n"
    "v_mfma_f32_16x16x32_bf16 v[200:203], v[204:207], v[60:63], v[200:203]\n"
    "ds_read_b128 v[16:19], v223 offset:0\n"
    "v_mfma_f32_16x16x32_bf16 v[200:203], v[208:211], v[64:67], v[200:203]\n"
    "ds_read_b128 v[20:23], v223 offset:1024\n"
    "v_mfma_f32_16x16x32_bf16 v[200:203], v[208:211], v[68:71], v[200:203]\n"
    "ds_read_b128 v[24:27], v223 offset:2048\n"
    "v_mfma_f32_16x16x32_bf16 v[200:203], v[208:211], v[72:75], v[200:203]\n"
    "ds_read_b128 v[28:31], v223 offset:3072\n"
    "v_mfma_f32_16x16x32_bf16 v[200:203], v[208:211], v[76:79], v[200:203]\n"
    "ds_read_b128 v[32:35], v224 offset:0\n"
    "v_mfma_f32_16x16x32_bf16 v[168:171], a[128:131], v[64:67], 0\n"
    "ds_read_b128 v[36:39], v224 offset:1024\n"
    "s_waitcnt lgkmcnt(6)\n"
    "v_add_f32 v218, v212, v213\n"
    "v_mfma_f32_16x16x32_bf16 v[168:171], a[160:163], v[68:71], v[168:171]\n"
    "ds_read_b128 v[40:43], v224 offset:2048\n"
    "v_add_f32 v219, v214, v215\n"
    "v_mfma_f32_16x16x32_bf16 v[168:171], a[192:195], v[72:75], v[168:171]\n"
    "ds_read_b128 v[44:47], v224 offset:3072\n"
    "v_add_f32 v218, v218, v219\n"
    "v_mfma_f32_16x16x32_bf16 v[168:171], a[224:227], v[76:79], v[168:171]\n"
    "ds_write_b32 v226, v200 offset:0\n"
    "v_mfma_f32_16x16x32_bf16 v[172:175], a[132:135], v[64:67], 0\n"
    "ds_read_b128 v[80:83], v225 offset:0\n"
    "v_mfma_f32_16x16x32_bf16 v[172:175], a[164:167], v[68:71], v[172:175]\n"
    "ds_read_b128 v[84:87], v225 offset:1024\n"
    "v_mfma_f32_16x16x32_bf16 v[172:175], a[196:199], v[72:75], v[172:175]\n"
    "ds_read_b128 v[88:91], v225 offset:2048\n"
    "v_mfma_f32_16x16x32_bf16 v[172:175], a[228:231], v[76:79], v[172:175]\n"
    "ds_read_b128 v[92:95], v225 offset:3072\n"
    "s_waitcnt lgkmcnt(7)\n"
    "v_mfma_f32_16x16x32_bf16 v[160:163], a[136:139], v[16:19], v[160:163]\n"
    "ds_read_b128 v[96:99], v223 offset:4096\n"
    "v_mfma_f32_16x16x32_bf16 v[160:163], a[168:171], v[20:23], v[160:163]\n"
    "ds_read_b128 v[100:103], v223 offset:5120\n"
    "v_mfma_f32_16x16x32_bf16 v[160:163], a[200:203], v[24:27], v[160:163]\n"
    "ds_read_b128 v[104:107], v223 offset:6144\n"
    "v_mfma_f32_16x16x32_bf16 v[160:163], a[232:235], v[28:31], v[160:163]\n"
    "ds_read_b128 v[108:111], v223 offset:7168\n"
    "v_mfma_f32_16x16x32_bf16 v[164:167], a[140:143], v[16:19], v[164:167]\n"
    "ds_read_b128 v[112:115], v224 offset:4096\n"
    "v_mfma_f32_16x16x32_bf16 v[164:167], a[172:175], v[20:23], v[164:167]\n"
    "ds_read_b128 v[116:119], v224 offset:5120\n"
    "v_mfma_f32_16x16x32_bf16 v[164:167], a[204:207], v[24:27], v[164:167]\n"
    "ds_read_b128 v[120:123], v224 offset:6144\n"
    "v_mfma_f32_16x16x32_bf16 v[164:167], a[236:239], v[28:31], v[164:167]\n"
    "ds_read_b128 v[124:127], v224 offset:7168\n"
    "v_mfma_f32_16x16x32_bf16 v[160:163], a[144:147], v[32:35], v[160:163]\n"
    "s_waitcnt lgkmcnt(6)\n"
    "ds_read_b128 v[128:131], v225 offset:4096\n"
    "v_mfma_f32_16x16x32_bf16 v[160:163], a[176:179], v[36:39], v[160:163]\n"
    "ds_read_b128 v[132:135], v225 offset:5120\n"
    "v_mfma_f32_16x16x32_bf16 v[160:163], a[208:211], v[40:43], v[160:163]\n"
    "ds_read_b128 v[136:139], v225 offset:6144\n"
    "v_mfma_f32_16x16x32_bf16 v[160:163], a[240:243], v[44:47], v[160:163]\n"
    "ds_read_b128 v[140:143], v225 offset:7168\n"
    "v_mfma_f32_16x16x32_bf16 v[164:167], a[148:151], v[32:35], v[164:167]\n"
    "v_rcp_f32 v216, v218\n"
    "v_mfma_f32_16x16x32_bf16 v[164:167], a[180:183], v[36:39], v[164:167]\n"
    "v_subrev_u32 v219, s63, v218\n"
    "v_mov_b32 v217, v216\n"
    "v_mfma_f32_16x16x32_bf16 v[164:167], a[212:215], v[40:43], v[164:167]\n"
    "v_cmp_le_u32 vcc, s64, v219\n"
    "v_mfma_f32_16x16x32_bf16 v[164:167], a[244:247], v[44:47], v[164:167]\n"
    "v_permlane16_swap_b32 v216, v217\n"
    "s_waitcnt lgkmcnt(10)\n"
    "s_barrier\n"
    "v_mfma_f32_16x16x32_bf16 v[160:163], a[152:155], v[80:83], v[160:163]\n"
    "ds_read_b128 v[212:215], v227 offset:0\n"
    "s_cmp_lg_u64 vcc, 0\n"
    "s_addc_u32 s57, s57, s57\n"
    "v_mfma_f32_16x16x32_bf16 v[160:163], a[184:187], v[84:87], v[160:163]\n"
    "v_mul_f32 v176, v176, v216\n"
    "v_mul_f32 v177, v177, v216\n"
    "v_mfma_f32_16x16x32_bf16 v[160:163], a[216:219], v[88:91], v[160:163]\n"
    "v_mul_f32 v178, v178, v216\n"
    "v_mul_f32 v179, v179, v216\n"
    "v_mfma_f32_16x16x32_bf16 v[160:163], a[248:251], v[92:95], v[160:163]\n"
    "v_mul_f32 v180, v180, v216\n"
    "v_mul_f32 v181, v181, v216\n"
    "v_mfma_f32_16x16x32_bf16 v[164:167], a[156:159], v[80:83], v[164:167]\n"
    "v_mul_f32 v182, v182, v216\n"
    "v_mul_f32 v183, v183, v216\n"
    "v_mfma_f32_16x16x32_bf16 v[164:167], a[188:191], v[84:87], v[164:167]\n"
    "v_cvt_pk_bf16_f32 v192, v176, v177\n"
    "v_cvt_pk_bf16_f32 v193, v178, v179\n"
    "v_mfma_f32_16x16x32_bf16 v[164:167], a[220:223], v[88:91], v[164:167]\n"
    "v_cvt_pk_bf16_f32 v194, v180, v181\n"
    "v_cvt_pk_bf16_f32 v195, v182, v183\n"
    "v_mfma_f32_16x16x32_bf16 v[164:167], a[252:255], v[92:95], v[164:167]\n"
    "buffer_store_dwordx4 v[192:195], v229, s[52:55], s56 offen offset:0 sc0 sc1\n"
    "v_mfma_f32_16x16x32_bf16 v[168:171], a[136:139], v[96:99], v[168:171]\n"
    "v_mul_f32 v184, v184, v217\n"
    "v_mul_f32 v185, v185, v217\n"
    "v_mfma_f32_16x16x32_bf16 v[168:171], a[168:171], v[100:103], v[168:171]\n"
    "v_mul_f32 v186, v186, v217\n"
    "v_mul_f32 v187, v187, v217\n"
    "s_waitcnt lgkmcnt(0)\n"
    "v_mfma_f32_16x16x32_bf16 v[168:171], a[200:203], v[104:107], v[168:171]\n"
    "v_mul_f32 v188, v188, v217\n"
    "v_mul_f32 v189, v189, v217\n"
    "v_mfma_f32_16x16x32_bf16 v[168:171], a[232:235], v[108:111], v[168:171]\n"
    "v_mul_f32 v190, v190, v217\n"
    "v_mul_f32 v191, v191, v217\n"
    "v_mfma_f32_16x16x32_bf16 v[172:175], a[140:143], v[96:99], v[172:175]\n"
    "v_cvt_pk_bf16_f32 v196, v184, v185\n"
    "v_cvt_pk_bf16_f32 v197, v186, v187\n"
    "v_mfma_f32_16x16x32_bf16 v[172:175], a[172:175], v[100:103], v[172:175]\n"
    "v_cvt_pk_bf16_f32 v198, v188, v189\n"
    "v_cvt_pk_bf16_f32 v199, v190, v191\n"
    "v_mfma_f32_16x16x32_bf16 v[172:175], a[204:207], v[104:107], v[172:175]\n"
    "buffer_store_dwordx4 v[196:199], v230, s[52:55], s56 offen offset:0 sc0 sc1\n"
    "v_mfma_f32_16x16x32_bf16 v[172:175], a[236:239], v[108:111], v[172:175]\n"
    "s_nop 0\n"
    "s_add_u32 s56, s56, 0x2000\n"
    "v_add_f32 v218, v212, v213\n"
    "v_mfma_f32_16x16x32_bf16 v[168:171], a[144:147], v[112:115], v[168:171]\n"
    "v_add_f32 v219, v214, v215\n"
    "v_add_f32 v218, v218, v219\n"
    "v_mfma_f32_16x16x32_bf16 v[168:171], a[176:179], v[116:119], v[168:171]\n"
    "v_rcp_f32 v216, v218\n"
    "v_mfma_f32_16x16x32_bf16 v[168:171], a[208:211], v[120:123], v[168:171]\n"
    "v_subrev_u32 v219, s63, v218\n"
    "v_mov_b32 v217, v216\n"
    "v_mfma_f32_16x16x32_bf16 v[168:171], a[240:243], v[124:127], v[168:171]\n"
    "v_cmp_le_u32 vcc, s64, v219\n"
    "v_mfma_f32_16x16x32_bf16 v[172:175], a[148:151], v[112:115], v[172:175]\n"
    "v_permlane16_swap_b32 v216, v217\n"
    "v_mfma_f32_16x16x32_bf16 v[172:175], a[180:183], v[116:119], v[172:175]\n"
    "s_cmp_lg_u64 vcc, 0\n"
    "s_addc_u32 s57, s57, s57\n"
    "v_mfma_f32_16x16x32_bf16 v[172:175], a[212:215], v[120:123], v[172:175]\n"
    "v_mul_f32 v160, v160, v216\n"
    "v_mul_f32 v161, v161, v216\n"
    "v_mfma_f32_16x16x32_bf16 v[172:175], a[244:247], v[124:127], v[172:175]\n"
    "v_mul_f32 v162, v162, v216\n"
    "v_mul_f32 v163, v163, v216\n"
    "v_mfma_f32_16x16x32_bf16 v[168:171], a[152:155], v[128:131], v[168:171]\n"
    "v_mul_f32 v164, v164, v216\n"
    "v_mul_f32 v165, v165, v216\n"
    "v_mfma_f32_16x16x32_bf16 v[168:171], a[184:187], v[132:135], v[168:171]\n"
    "v_mul_f32 v166, v166, v216\n"
    "v_mul_f32 v167, v167, v216\n"
    "v_mfma_f32_16x16x32_bf16 v[168:171], a[216:219], v[136:139], v[168:171]\n"
    "v_cvt_pk_bf16_f32 v192, v160, v161\n"
    "v_cvt_pk_bf16_f32 v193, v162, v163\n"
    "v_mfma_f32_16x16x32_bf16 v[168:171], a[248:251], v[140:143], v[168:171]\n"
    "v_cvt_pk_bf16_f32 v194, v164, v165\n"
    "v_cvt_pk_bf16_f32 v195, v166, v167\n"
    "v_mfma_f32_16x16x32_bf16 v[172:175], a[156:159], v[128:131], v[172:175]\n"
    "buffer_store_dwordx4 v[192:195], v229, s[52:55], s56 offen offset:0 sc0 sc1\n"
    "v_mfma_f32_16x16x32_bf16 v[172:175], a[188:191], v[132:135], v[172:175]\n"
    "v_mfma_f32_16x16x32_bf16 v[172:175], a[220:223], v[136:139], v[172:175]\n"
    "v_mfma_f32_16x16x32_bf16 v[172:175], a[252:255], v[140:143], v[172:175]\n"
    "s_nop 0\n"
    "v_mul_f32 v168, v168, v217\n"
    "v_mul_f32 v169, v169, v217\n"
    "v_mul_f32 v170, v170, v217\n"
    "v_mul_f32 v171, v171, v217\n"
    "s_nop 2\n"
    "v_mul_f32 v172, v172, v217\n"
    "v_mul_f32 v173, v173, v217\n"
    "v_mul_f32 v174, v174, v217\n"
    "v_mul_f32 v175, v175, v217\n"
    "v_cvt_pk_bf16_f32 v196, v168, v169\n"
    "v_cvt_pk_bf16_f32 v197, v170, v171\n"
    "v_cvt_pk_bf16_f32 v198, v172, v173\n"
    "v_cvt_pk_bf16_f32 v199, v174, v175\n"
    "buffer_store_dwordx4 v[196:199], v230, s[52:55], s56 offen offset:0 sc0 sc1\n"
    "s_cmp_lg_u32 s57, 0\n"
    "s_cbranch_scc1 .Lgqa_slow%=\n"
    "s_endpgm\n"
    ".Lgqa_slow%=:\n"
    "s_cmp_eq_u32 s57, 0\n"
    "s_cbranch_scc1 .Lgqa_end%=\n"
    "s_waitcnt vmcnt(0) lgkmcnt(0)\n"
    "s_barrier\n"
    "s_ff1_i32_b32 s68, s57\n"
    "s_bitset0_b32 s57, s68\n"
    "s_sub_u32 s68, 30, s68\n"
    "s_lshl_b32 s68, s68, 13\n"
    "s_xor_b32 m0, s66, 0x2000\n"
    "s_nop 0\n"
    "buffer_load_dwordx4 v228, s[40:43], s68 offen offset:0 lds\n"
    "buffer_load_dwordx4 v228, s[40:43], s68 offen offset:1024 lds\n"
    "s_waitcnt vmcnt(0) lgkmcnt(0)\n"
    "s_barrier\n"
    "ds_read_b128 v[16:19], v221 offset:0\n"
    "ds_read_b128 v[20:23], v221 offset:256\n"
    "ds_read_b128 v[24:27], v221 offset:512\n"
    "ds_read_b128 v[28:31], v221 offset:768\n"
    "ds_read_b128 v[32:35], v221 offset:32768\n"
    "ds_read_b128 v[36:39], v221 offset:33024\n"
    "ds_read_b128 v[40:43], v221 offset:33280\n"
    "ds_read_b128 v[44:47], v221 offset:33536\n"
    "s_waitcnt lgkmcnt(7)\n"
    "v_mfma_f32_16x16x32_bf16 v[112:115], a[0:3], v[16:19], 0\n"
    "s_waitcnt lgkmcnt(6)\n"
    "v_mfma_f32_16x16x32_bf16 v[112:115], a[4:7], v[20:23], v[112:115]\n"
    "s_waitcnt lgkmcnt(5)\n"
    "v_mfma_f32_16x16x32_bf16 v[112:115], a[8:11], v[24:27], v[112:115]\n"
    "s_waitcnt lgkmcnt(4)\n"
    "v_mfma_f32_16x16x32_bf16 v[112:115], a[12:15], v[28:31], v[112:115]\n"
    "v_mfma_f32_16x16x32_bf16 v[116:119], a[16:19], v[16:19], 0\n"
    "v_mfma_f32_16x16x32_bf16 v[116:119], a[20:23], v[20:23], v[116:119]\n"
    "v_mfma_f32_16x16x32_bf16 v[116:119], a[24:27], v[24:27], v[116:119]\n"
    "v_mfma_f32_16x16x32_bf16 v[116:119], a[28:31], v[28:31], v[116:119]\n"
    "v_mfma_f32_16x16x32_bf16 v[120:123], a[32:35], v[16:19], 0\n"
    "v_mfma_f32_16x16x32_bf16 v[120:123], a[36:39], v[20:23], v[120:123]\n"
    "v_mfma_f32_16x16x32_bf16 v[120:123], a[40:43], v[24:27], v[120:123]\n"
    "v_mfma_f32_16x16x32_bf16 v[120:123], a[44:47], v[28:31], v[120:123]\n"
    "v_mfma_f32_16x16x32_bf16 v[124:127], a[48:51], v[16:19], 0\n"
    "v_mfma_f32_16x16x32_bf16 v[124:127], a[52:55], v[20:23], v[124:127]\n"
    "v_mfma_f32_16x16x32_bf16 v[124:127], a[56:59], v[24:27], v[124:127]\n"
    "v_mfma_f32_16x16x32_bf16 v[124:127], a[60:63], v[28:31], v[124:127]\n"
    "v_mfma_f32_16x16x32_bf16 v[128:131], a[64:67], v[16:19], 0\n"
    "v_mfma_f32_16x16x32_bf16 v[128:131], a[68:71], v[20:23], v[128:131]\n"
    "v_mfma_f32_16x16x32_bf16 v[128:131], a[72:75], v[24:27], v[128:131]\n"
    "v_mfma_f32_16x16x32_bf16 v[128:131], a[76:79], v[28:31], v[128:131]\n"
    "v_mfma_f32_16x16x32_bf16 v[132:135], a[80:83], v[16:19], 0\n"
    "v_mfma_f32_16x16x32_bf16 v[132:135], a[84:87], v[20:23], v[132:135]\n"
    "v_mfma_f32_16x16x32_bf16 v[132:135], a[88:91], v[24:27], v[132:135]\n"
    "v_mfma_f32_16x16x32_bf16 v[132:135], a[92:95], v[28:31], v[132:135]\n"
    "v_mfma_f32_16x16x32_bf16 v[136:139], a[96:99], v[16:19], 0\n"
    "v_mfma_f32_16x16x32_bf16 v[136:139], a[100:103], v[20:23], v[136:139]\n"
    "v_mfma_f32_16x16x32_bf16 v[136:139], a[104:107], v[24:27], v[136:139]\n"
    "v_mfma_f32_16x16x32_bf16 v[136:139], a[108:111], v[28:31], v[136:139]\n"
    "v_mfma_f32_16x16x32_bf16 v[140:143], a[112:115], v[16:19], 0\n"
    "v_mfma_f32_16x16x32_bf16 v[140:143], a[116:119], v[20:23], v[140:143]\n"
    "v_mfma_f32_16x16x32_bf16 v[140:143], a[120:123], v[24:27], v[140:143]\n"
    "v_mfma_f32_16x16x32_bf16 v[140:143], a[124:127], v[28:31], v[140:143]\n"
    "s_waitcnt lgkmcnt(0)\n"
    "v_mfma_f32_16x16x32_bf16 v[144:147], a[0:3], v[32:35], 0\n"
    "v_mfma_f32_16x16x32_bf16 v[144:147], a[4:7], v[36:39], v[144:147]\n"
    "v_mfma_f32_16x16x32_bf16 v[144:147], a[8:11], v[40:43], v[144:147]\n"
    "v_mfma_f32_16x16x32_bf16 v[144:147], a[12:15], v[44:47], v[144:147]\n"
    "v_mfma_f32_16x16x32_bf16 v[148:151], a[16:19], v[32:35], 0\n"
    "v_mfma_f32_16x16x32_bf16 v[148:151], a[20:23], v[36:39], v[148:151]\n"
    "v_mfma_f32_16x16x32_bf16 v[148:151], a[24:27], v[40:43], v[148:151]\n"
    "v_mfma_f32_16x16x32_bf16 v[148:151], a[28:31], v[44:47], v[148:151]\n"
    "v_mfma_f32_16x16x32_bf16 v[152:155], a[32:35], v[32:35], 0\n"
    "v_mfma_f32_16x16x32_bf16 v[152:155], a[36:39], v[36:39], v[152:155]\n"
    "v_mfma_f32_16x16x32_bf16 v[152:155], a[40:43], v[40:43], v[152:155]\n"
    "v_mfma_f32_16x16x32_bf16 v[152:155], a[44:47], v[44:47], v[152:155]\n"
    "v_mfma_f32_16x16x32_bf16 v[156:159], a[48:51], v[32:35], 0\n"
    "v_mfma_f32_16x16x32_bf16 v[156:159], a[52:55], v[36:39], v[156:159]\n"
    "v_mfma_f32_16x16x32_bf16 v[156:159], a[56:59], v[40:43], v[156:159]\n"
    "v_mfma_f32_16x16x32_bf16 v[156:159], a[60:63], v[44:47], v[156:159]\n"
    "v_mfma_f32_16x16x32_bf16 v[160:163], a[64:67], v[32:35], 0\n"
    "v_mfma_f32_16x16x32_bf16 v[160:163], a[68:71], v[36:39], v[160:163]\n"
    "v_mfma_f32_16x16x32_bf16 v[160:163], a[72:75], v[40:43], v[160:163]\n"
    "v_mfma_f32_16x16x32_bf16 v[160:163], a[76:79], v[44:47], v[160:163]\n"
    "v_mfma_f32_16x16x32_bf16 v[164:167], a[80:83], v[32:35], 0\n"
    "v_mfma_f32_16x16x32_bf16 v[164:167], a[84:87], v[36:39], v[164:167]\n"
    "v_mfma_f32_16x16x32_bf16 v[164:167], a[88:91], v[40:43], v[164:167]\n"
    "v_mfma_f32_16x16x32_bf16 v[164:167], a[92:95], v[44:47], v[164:167]\n"
    "v_mfma_f32_16x16x32_bf16 v[168:171], a[96:99], v[32:35], 0\n"
    "v_mfma_f32_16x16x32_bf16 v[168:171], a[100:103], v[36:39], v[168:171]\n"
    "v_mfma_f32_16x16x32_bf16 v[168:171], a[104:107], v[40:43], v[168:171]\n"
    "v_mfma_f32_16x16x32_bf16 v[168:171], a[108:111], v[44:47], v[168:171]\n"
    "v_mfma_f32_16x16x32_bf16 v[172:175], a[112:115], v[32:35], 0\n"
    "v_mfma_f32_16x16x32_bf16 v[172:175], a[116:119], v[36:39], v[172:175]\n"
    "v_mfma_f32_16x16x32_bf16 v[172:175], a[120:123], v[40:43], v[172:175]\n"
    "v_mfma_f32_16x16x32_bf16 v[172:175], a[124:127], v[44:47], v[172:175]\n"
    "v_max_f32 v0, v112, v113\n"
    "v_max3_f32 v0, v0, v114, v115\n"
    "v_max3_f32 v0, v0, v116, v117\n"
    "v_max3_f32 v0, v0, v118, v119\n"
    "v_max3_f32 v0, v0, v120, v121\n"
    "v_max3_f32 v0, v0, v122, v123\n"
    "v_max3_f32 v0, v0, v124, v125\n"
    "v_max3_f32 v0, v0, v126, v127\n"
    "v_max3_f32 v0, v0, v128, v129\n"
    "v_max3_f32 v0, v0, v130, v131\n"
    "v_max3_f32 v0, v0, v132, v133\n"
    "v_max3_f32 v0, v0, v134, v135\n"
    "v_max3_f32 v0, v0, v136, v137\n"
    "v_max3_f32 v0, v0, v138, v139\n"
    "v_max3_f32 v0, v0, v140, v141\n"
    "v_max3_f32 v0, v0, v142, v143\n"
    "v_max_f32 v1, v144, v145\n"
    "v_max3_f32 v1, v1, v146, v147\n"
    "v_max3_f32 v1, v1, v148, v149\n"
    "v_max3_f32 v1, v1, v150, v151\n"
    "v_max3_f32 v1, v1, v152, v153\n"
    "v_max3_f32 v1, v1, v154, v155\n"
    "v_max3_f32 v1, v1, v156, v157\n"
    "v_max3_f32 v1, v1, v158, v159\n"
    "v_max3_f32 v1, v1, v160, v161\n"
    "v_max3_f32 v1, v1, v162, v163\n"
    "v_max3_f32 v1, v1, v164, v165\n"
    "v_max3_f32 v1, v1, v166, v167\n"
    "v_max3_f32 v1, v1, v168, v169\n"
    "v_max3_f32 v1, v1, v170, v171\n"
    "v_max3_f32 v1, v1, v172, v173\n"
    "v_max3_f32 v1, v1, v174, v175\n"
    "s_nop 1\n"
    "v_permlane16_swap_b32 v0, v1\n"
    "v_max_f32 v0, v0, v1\n"
    "v_mov_b32 v1, v0\n"
    "s_nop 1\n"
    "v_permlane32_swap_b32 v0, v1\n"
    "v_max_f32 v0, v0, v1\n"
    "ds_write_b32 v226, v0 offset:0\n"
    "s_waitcnt lgkmcnt(0)\n"
    "s_barrier\n"
    "ds_read_b128 v[212:215], v227 offset:0\n"
    "s_waitcnt lgkmcnt(0)\n"
    "v_max3_f32 v0, v212, v213, v214\n"
    "v_max_f32 v0, v0, v215\n"
    "v_mov_b32 v1, v0\n"
    "s_nop 1\n"
    "v_permlane16_swap_b32 v0, v1\n"
    "v_sub_f32 v112, v112, v0\n"
    "v_sub_f32 v113, v113, v0\n"
    "v_sub_f32 v114, v114, v0\n"
    "v_sub_f32 v115, v115, v0\n"
    "v_sub_f32 v116, v116, v0\n"
    "v_sub_f32 v117, v117, v0\n"
    "v_sub_f32 v118, v118, v0\n"
    "v_sub_f32 v119, v119, v0\n"
    "v_sub_f32 v120, v120, v0\n"
    "v_sub_f32 v121, v121, v0\n"
    "v_sub_f32 v122, v122, v0\n"
    "v_sub_f32 v123, v123, v0\n"
    "v_sub_f32 v124, v124, v0\n"
    "v_sub_f32 v125, v125, v0\n"
    "v_sub_f32 v126, v126, v0\n"
    "v_sub_f32 v127, v127, v0\n"
    "v_sub_f32 v128, v128, v0\n"
    "v_sub_f32 v129, v129, v0\n"
    "v_sub_f32 v130, v130, v0\n"
    "v_sub_f32 v131, v131, v0\n"
    "v_sub_f32 v132, v132, v0\n"
    "v_sub_f32 v133, v133, v0\n"
    "v_sub_f32 v134, v134, v0\n"
    "v_sub_f32 v135, v135, v0\n"
    "v_sub_f32 v136, v136, v0\n"
    "v_sub_f32 v137, v137, v0\n"
    "v_sub_f32 v138, v138, v0\n"
    "v_sub_f32 v139, v139, v0\n"
    "v_sub_f32 v140, v140, v0\n"
    "v_sub_f32 v141, v141, v0\n"
    "v_sub_f32 v142, v142, v0\n"
    "v_sub_f32 v143, v143, v0\n"
    "v_sub_f32 v144, v144, v1\n"
    "v_sub_f32 v145, v145, v1\n"
    "v_sub_f32 v146, v146, v1\n"
    "v_sub_f32 v147, v147, v1\n"
    "v_sub_f32 v148, v148, v1\n"
    "v_sub_f32 v149, v149, v1\n"
    "v_sub_f32 v150, v150, v1\n"
    "v_sub_f32 v151, v151, v1\n"
    "v_sub_f32 v152, v152, v1\n"
    "v_sub_f32 v153, v153, v1\n"
    "v_sub_f32 v154, v154, v1\n"
    "v_sub_f32 v155, v155, v1\n"
    "v_sub_f32 v156, v156, v1\n"
    "v_sub_f32 v157, v157, v1\n"
    "v_sub_f32 v158, v158, v1\n"
    "v_sub_f32 v159, v159, v1\n"
    "v_sub_f32 v160, v160, v1\n"
    "v_sub_f32 v161, v161, v1\n"
    "v_sub_f32 v162, v162, v1\n"
    "v_sub_f32 v163, v163, v1\n"
    "v_sub_f32 v164, v164, v1\n"
    "v_sub_f32 v165, v165, v1\n"
    "v_sub_f32 v166, v166, v1\n"
    "v_sub_f32 v167, v167, v1\n"
    "v_sub_f32 v168, v168, v1\n"
    "v_sub_f32 v169, v169, v1\n"
    "v_sub_f32 v170, v170, v1\n"
    "v_sub_f32 v171, v171, v1\n"
    "v_sub_f32 v172, v172, v1\n"
    "v_sub_f32 v173, v173, v1\n"
    "v_sub_f32 v174, v174, v1\n"
    "v_sub_f32 v175, v175, v1\n"
    "v_exp_f32 v112, v112\n"
    "v_exp_f32 v113, v113\n"
    "v_exp_f32 v114, v114\n"
    "v_exp_f32 v115, v115\n"
    "v_exp_f32 v116, v116\n"
    "v_exp_f32 v117, v117\n"
    "v_exp_f32 v118, v118\n"
    "v_exp_f32 v119, v119\n"
    "v_exp_f32 v120, v120\n"
    "v_exp_f32 v121, v121\n"
    "v_exp_f32 v122, v122\n"
    "v_exp_f32 v123, v123\n"
    "v_exp_f32 v124, v124\n"
    "v_exp_f32 v125, v125\n"
    "v_exp_f32 v126, v126\n"
    "v_exp_f32 v127, v127\n"
    "v_exp_f32 v128, v128\n"
    "v_exp_f32 v129, v129\n"
    "v_exp_f32 v130, v130\n"
    "v_exp_f32 v131, v131\n"
    "v_exp_f32 v132, v132\n"
    "v_exp_f32 v133, v133\n"
    "v_exp_f32 v134, v134\n"
    "v_exp_f32 v135, v135\n"
    "v_exp_f32 v136, v136\n"
    "v_exp_f32 v137, v137\n"
    "v_exp_f32 v138, v138\n"
    "v_exp_f32 v139, v139\n"
    "v_exp_f32 v140, v140\n"
    "v_exp_f32 v141, v141\n"
    "v_exp_f32 v142, v142\n"
    "v_exp_f32 v143, v143\n"
    "v_exp_f32 v144, v144\n"
    "v_exp_f32 v145, v145\n"
    "v_exp_f32 v146, v146\n"
    "v_exp_f32 v147, v147\n"
    "v_exp_f32 v148, v148\n"
    "v_exp_f32 v149, v149\n"
    "v_exp_f32 v150, v150\n"
    "v_exp_f32 v151, v151\n"
    "v_exp_f32 v152, v152\n"
    "v_exp_f32 v153, v153\n"
    "v_exp_f32 v154, v154\n"
    "v_exp_f32 v155, v155\n"
    "v_exp_f32 v156, v156\n"
    "v_exp_f32 v157, v157\n"
    "v_exp_f32 v158, v158\n"
    "v_exp_f32 v159, v159\n"
    "v_exp_f32 v160, v160\n"
    "v_exp_f32 v161, v161\n"
    "v_exp_f32 v162, v162\n"
    "v_exp_f32 v163, v163\n"
    "v_exp_f32 v164, v164\n"
    "v_exp_f32 v165, v165\n"
    "v_exp_f32 v166, v166\n"
    "v_exp_f32 v167, v167\n"
    "v_exp_f32 v168, v168\n"
    "v_exp_f32 v169, v169\n"
    "v_exp_f32 v170, v170\n"
    "v_exp_f32 v171, v171\n"
    "v_exp_f32 v172, v172\n"
    "v_exp_f32 v173, v173\n"
    "v_exp_f32 v174, v174\n"
    "v_exp_f32 v175, v175\n"
    "v_cvt_pk_bf16_f32 v48, v112, v113\n"
    "v_cvt_pk_bf16_f32 v49, v114, v115\n"
    "v_cvt_pk_bf16_f32 v50, v116, v117\n"
    "v_cvt_pk_bf16_f32 v51, v118, v119\n"
    "v_cvt_pk_bf16_f32 v52, v120, v121\n"
    "v_cvt_pk_bf16_f32 v53, v122, v123\n"
    "v_cvt_pk_bf16_f32 v54, v124, v125\n"
    "v_cvt_pk_bf16_f32 v55, v126, v127\n"
    "v_cvt_pk_bf16_f32 v56, v128, v129\n"
    "v_cvt_pk_bf16_f32 v57, v130, v131\n"
    "v_cvt_pk_bf16_f32 v58, v132, v133\n"
    "v_cvt_pk_bf16_f32 v59, v134, v135\n"
    "v_cvt_pk_bf16_f32 v60, v136, v137\n"
    "v_cvt_pk_bf16_f32 v61, v138, v139\n"
    "v_cvt_pk_bf16_f32 v62, v140, v141\n"
    "v_cvt_pk_bf16_f32 v63, v142, v143\n"
    "v_cvt_pk_bf16_f32 v64, v144, v145\n"
    "v_cvt_pk_bf16_f32 v65, v146, v147\n"
    "v_cvt_pk_bf16_f32 v66, v148, v149\n"
    "v_cvt_pk_bf16_f32 v67, v150, v151\n"
    "v_cvt_pk_bf16_f32 v68, v152, v153\n"
    "v_cvt_pk_bf16_f32 v69, v154, v155\n"
    "v_cvt_pk_bf16_f32 v70, v156, v157\n"
    "v_cvt_pk_bf16_f32 v71, v158, v159\n"
    "v_cvt_pk_bf16_f32 v72, v160, v161\n"
    "v_cvt_pk_bf16_f32 v73, v162, v163\n"
    "v_cvt_pk_bf16_f32 v74, v164, v165\n"
    "v_cvt_pk_bf16_f32 v75, v166, v167\n"
    "v_cvt_pk_bf16_f32 v76, v168, v169\n"
    "v_cvt_pk_bf16_f32 v77, v170, v171\n"
    "v_cvt_pk_bf16_f32 v78, v172, v173\n"
    "v_cvt_pk_bf16_f32 v79, v174, v175\n"
    "ds_write_b128 v222, v[48:51] offset:0\n"
    "ds_write_b128 v222, v[52:55] offset:1024\n"
    "ds_write_b128 v222, v[56:59] offset:2048\n"
    "ds_write_b128 v222, v[60:63] offset:3072\n"
    "ds_write_b128 v222, v[64:67] offset:4096\n"
    "ds_write_b128 v222, v[68:71] offset:5120\n"
    "ds_write_b128 v222, v[72:75] offset:6144\n"
    "ds_write_b128 v222, v[76:79] offset:7168\n"
    "v_mfma_f32_16x16x32_bf16 v[200:203], v[204:207], v[48:51], 0\n"
    "v_mfma_f32_16x16x32_bf16 v[200:203], v[204:207], v[52:55], v[200:203]\n"
    "v_mfma_f32_16x16x32_bf16 v[200:203], v[204:207], v[56:59], v[200:203]\n"
    "v_mfma_f32_16x16x32_bf16 v[200:203], v[204:207], v[60:63], v[200:203]\n"
    "v_mfma_f32_16x16x32_bf16 v[200:203], v[208:211], v[64:67], v[200:203]\n"
    "v_mfma_f32_16x16x32_bf16 v[200:203], v[208:211], v[68:71], v[200:203]\n"
    "v_mfma_f32_16x16x32_bf16 v[200:203], v[208:211], v[72:75], v[200:203]\n"
    "v_mfma_f32_16x16x32_bf16 v[200:203], v[208:211], v[76:79], v[200:203]\n"
    "s_nop 7\n"
    "ds_write_b32 v226, v200 offset:512\n"
    "s_waitcnt lgkmcnt(0)\n"
    "s_barrier\n"
    "v_mfma_f32_16x16x32_bf16 v[176:179], a[128:131], v[48:51], 0\n"
    "v_mfma_f32_16x16x32_bf16 v[176:179], a[160:163], v[52:55], v[176:179]\n"
    "v_mfma_f32_16x16x32_bf16 v[176:179], a[192:195], v[56:59], v[176:179]\n"
    "v_mfma_f32_16x16x32_bf16 v[176:179], a[224:227], v[60:63], v[176:179]\n"
    "v_mfma_f32_16x16x32_bf16 v[180:183], a[132:135], v[48:51], 0\n"
    "v_mfma_f32_16x16x32_bf16 v[180:183], a[164:167], v[52:55], v[180:183]\n"
    "v_mfma_f32_16x16x32_bf16 v[180:183], a[196:199], v[56:59], v[180:183]\n"
    "v_mfma_f32_16x16x32_bf16 v[180:183], a[228:231], v[60:63], v[180:183]\n"
    "v_mfma_f32_16x16x32_bf16 v[184:187], a[128:131], v[64:67], 0\n"
    "v_mfma_f32_16x16x32_bf16 v[184:187], a[160:163], v[68:71], v[184:187]\n"
    "v_mfma_f32_16x16x32_bf16 v[184:187], a[192:195], v[72:75], v[184:187]\n"
    "v_mfma_f32_16x16x32_bf16 v[184:187], a[224:227], v[76:79], v[184:187]\n"
    "v_mfma_f32_16x16x32_bf16 v[188:191], a[132:135], v[64:67], 0\n"
    "v_mfma_f32_16x16x32_bf16 v[188:191], a[164:167], v[68:71], v[188:191]\n"
    "v_mfma_f32_16x16x32_bf16 v[188:191], a[196:199], v[72:75], v[188:191]\n"
    "v_mfma_f32_16x16x32_bf16 v[188:191], a[228:231], v[76:79], v[188:191]\n"
    "ds_read_b128 v[80:83], v223 offset:0\n"
    "ds_read_b128 v[84:87], v223 offset:1024\n"
    "ds_read_b128 v[88:91], v223 offset:2048\n"
    "ds_read_b128 v[92:95], v223 offset:3072\n"
    "ds_read_b128 v[96:99], v223 offset:4096\n"
    "ds_read_b128 v[100:103], v223 offset:5120\n"
    "ds_read_b128 v[104:107], v223 offset:6144\n"
    "ds_read_b128 v[108:111], v223 offset:7168\n"
    "s_waitcnt lgkmcnt(7)\n"
    "v_mfma_f32_16x16x32_bf16 v[176:179], a[136:139], v[80:83], v[176:179]\n"
    "s_waitcnt lgkmcnt(6)\n"
    "v_mfma_f32_16x16x32_bf16 v[176:179], a[168:171], v[84:87], v[176:179]\n"
    "s_waitcnt lgkmcnt(5)\n"
    "v_mfma_f32_16x16x32_bf16 v[176:179], a[200:203], v[88:91], v[176:179]\n"
    "s_waitcnt lgkmcnt(4)\n"
    "v_mfma_f32_16x16x32_bf16 v[176:179], a[232:235], v[92:95], v[176:179]\n"
    "v_mfma_f32_16x16x32_bf16 v[180:183], a[140:143], v[80:83], v[180:183]\n"
    "v_mfma_f32_16x16x32_bf16 v[180:183], a[172:175], v[84:87], v[180:183]\n"
    "v_mfma_f32_16x16x32_bf16 v[180:183], a[204:207], v[88:91], v[180:183]\n"
    "v_mfma_f32_16x16x32_bf16 v[180:183], a[236:239], v[92:95], v[180:183]\n"
    "s_waitcnt lgkmcnt(0)\n"
    "v_mfma_f32_16x16x32_bf16 v[184:187], a[136:139], v[96:99], v[184:187]\n"
    "v_mfma_f32_16x16x32_bf16 v[184:187], a[168:171], v[100:103], v[184:187]\n"
    "v_mfma_f32_16x16x32_bf16 v[184:187], a[200:203], v[104:107], v[184:187]\n"
    "v_mfma_f32_16x16x32_bf16 v[184:187], a[232:235], v[108:111], v[184:187]\n"
    "v_mfma_f32_16x16x32_bf16 v[188:191], a[140:143], v[96:99], v[188:191]\n"
    "v_mfma_f32_16x16x32_bf16 v[188:191], a[172:175], v[100:103], v[188:191]\n"
    "v_mfma_f32_16x16x32_bf16 v[188:191], a[204:207], v[104:107], v[188:191]\n"
    "v_mfma_f32_16x16x32_bf16 v[188:191], a[236:239], v[108:111], v[188:191]\n"
    "ds_read_b128 v[80:83], v224 offset:0\n"
    "ds_read_b128 v[84:87], v224 offset:1024\n"
    "ds_read_b128 v[88:91], v224 offset:2048\n"
    "ds_read_b128 v[92:95], v224 offset:3072\n"
    "ds_read_b128 v[96:99], v224 offset:4096\n"
    "ds_read_b128 v[100:103], v224 offset:5120\n"
    "ds_read_b128 v[104:107], v224 offset:6144\n"
    "ds_read_b128 v[108:111], v224 offset:7168\n"
    "s_waitcnt lgkmcnt(7)\n"
    "v_mfma_f32_16x16x32_bf16 v[176:179], a[144:147], v[80:83], v[176:179]\n"
    "s_waitcnt lgkmcnt(6)\n"
    "v_mfma_f32_16x16x32_bf16 v[176:179], a[176:179], v[84:87], v[176:179]\n"
    "s_waitcnt lgkmcnt(5)\n"
    "v_mfma_f32_16x16x32_bf16 v[176:179], a[208:211], v[88:91], v[176:179]\n"
    "s_waitcnt lgkmcnt(4)\n"
    "v_mfma_f32_16x16x32_bf16 v[176:179], a[240:243], v[92:95], v[176:179]\n"
    "v_mfma_f32_16x16x32_bf16 v[180:183], a[148:151], v[80:83], v[180:183]\n"
    "v_mfma_f32_16x16x32_bf16 v[180:183], a[180:183], v[84:87], v[180:183]\n"
    "v_mfma_f32_16x16x32_bf16 v[180:183], a[212:215], v[88:91], v[180:183]\n"
    "v_mfma_f32_16x16x32_bf16 v[180:183], a[244:247], v[92:95], v[180:183]\n"
    "s_waitcnt lgkmcnt(0)\n"
    "v_mfma_f32_16x16x32_bf16 v[184:187], a[144:147], v[96:99], v[184:187]\n"
    "v_mfma_f32_16x16x32_bf16 v[184:187], a[176:179], v[100:103], v[184:187]\n"
    "v_mfma_f32_16x16x32_bf16 v[184:187], a[208:211], v[104:107], v[184:187]\n"
    "v_mfma_f32_16x16x32_bf16 v[184:187], a[240:243], v[108:111], v[184:187]\n"
    "v_mfma_f32_16x16x32_bf16 v[188:191], a[148:151], v[96:99], v[188:191]\n"
    "v_mfma_f32_16x16x32_bf16 v[188:191], a[180:183], v[100:103], v[188:191]\n"
    "v_mfma_f32_16x16x32_bf16 v[188:191], a[212:215], v[104:107], v[188:191]\n"
    "v_mfma_f32_16x16x32_bf16 v[188:191], a[244:247], v[108:111], v[188:191]\n"
    "ds_read_b128 v[80:83], v225 offset:0\n"
    "ds_read_b128 v[84:87], v225 offset:1024\n"
    "ds_read_b128 v[88:91], v225 offset:2048\n"
    "ds_read_b128 v[92:95], v225 offset:3072\n"
    "ds_read_b128 v[96:99], v225 offset:4096\n"
    "ds_read_b128 v[100:103], v225 offset:5120\n"
    "ds_read_b128 v[104:107], v225 offset:6144\n"
    "ds_read_b128 v[108:111], v225 offset:7168\n"
    "s_waitcnt lgkmcnt(7)\n"
    "v_mfma_f32_16x16x32_bf16 v[176:179], a[152:155], v[80:83], v[176:179]\n"
    "s_waitcnt lgkmcnt(6)\n"
    "v_mfma_f32_16x16x32_bf16 v[176:179], a[184:187], v[84:87], v[176:179]\n"
    "s_waitcnt lgkmcnt(5)\n"
    "v_mfma_f32_16x16x32_bf16 v[176:179], a[216:219], v[88:91], v[176:179]\n"
    "s_waitcnt lgkmcnt(4)\n"
    "v_mfma_f32_16x16x32_bf16 v[176:179], a[248:251], v[92:95], v[176:179]\n"
    "v_mfma_f32_16x16x32_bf16 v[180:183], a[156:159], v[80:83], v[180:183]\n"
    "v_mfma_f32_16x16x32_bf16 v[180:183], a[188:191], v[84:87], v[180:183]\n"
    "v_mfma_f32_16x16x32_bf16 v[180:183], a[220:223], v[88:91], v[180:183]\n"
    "v_mfma_f32_16x16x32_bf16 v[180:183], a[252:255], v[92:95], v[180:183]\n"
    "s_waitcnt lgkmcnt(0)\n"
    "v_mfma_f32_16x16x32_bf16 v[184:187], a[152:155], v[96:99], v[184:187]\n"
    "v_mfma_f32_16x16x32_bf16 v[184:187], a[184:187], v[100:103], v[184:187]\n"
    "v_mfma_f32_16x16x32_bf16 v[184:187], a[216:219], v[104:107], v[184:187]\n"
    "v_mfma_f32_16x16x32_bf16 v[184:187], a[248:251], v[108:111], v[184:187]\n"
    "v_mfma_f32_16x16x32_bf16 v[188:191], a[156:159], v[96:99], v[188:191]\n"
    "v_mfma_f32_16x16x32_bf16 v[188:191], a[188:191], v[100:103], v[188:191]\n"
    "v_mfma_f32_16x16x32_bf16 v[188:191], a[220:223], v[104:107], v[188:191]\n"
    "v_mfma_f32_16x16x32_bf16 v[188:191], a[252:255], v[108:111], v[188:191]\n"
    "ds_read_b128 v[212:215], v227 offset:512\n"
    "s_waitcnt lgkmcnt(0)\n"
    "v_add_f32 v218, v212, v213\n"
    "v_add_f32 v219, v214, v215\n"
    "v_add_f32 v218, v218, v219\n"
    "v_rcp_f32 v216, v218\n"
    "s_nop 0\n"
    "v_mov_b32 v217, v216\n"
    "s_nop 1\n"
    "v_permlane16_swap_b32 v216, v217\n"
    "v_mul_f32 v176, v176, v216\n"
    "v_mul_f32 v177, v177, v216\n"
    "v_mul_f32 v178, v178, v216\n"
    "v_mul_f32 v179, v179, v216\n"
    "v_mul_f32 v180, v180, v216\n"
    "v_mul_f32 v181, v181, v216\n"
    "v_mul_f32 v182, v182, v216\n"
    "v_mul_f32 v183, v183, v216\n"
    "v_mul_f32 v184, v184, v217\n"
    "v_mul_f32 v185, v185, v217\n"
    "v_mul_f32 v186, v186, v217\n"
    "v_mul_f32 v187, v187, v217\n"
    "v_mul_f32 v188, v188, v217\n"
    "v_mul_f32 v189, v189, v217\n"
    "v_mul_f32 v190, v190, v217\n"
    "v_mul_f32 v191, v191, v217\n"
    "v_cvt_pk_bf16_f32 v192, v176, v177\n"
    "v_cvt_pk_bf16_f32 v193, v178, v179\n"
    "v_cvt_pk_bf16_f32 v194, v180, v181\n"
    "v_cvt_pk_bf16_f32 v195, v182, v183\n"
    "v_cvt_pk_bf16_f32 v196, v184, v185\n"
    "v_cvt_pk_bf16_f32 v197, v186, v187\n"
    "v_cvt_pk_bf16_f32 v198, v188, v189\n"
    "v_cvt_pk_bf16_f32 v199, v190, v191\n"
    "s_add_u32 s68, s68, 0x4000\n"
    "buffer_store_dwordx4 v[192:195], v229, s[52:55], s68 offen offset:0\n"
    "buffer_store_dwordx4 v[196:199], v230, s[52:55], s68 offen offset:0\n"
    "s_waitcnt vmcnt(0) lgkmcnt(0)\n"
    "s_branch .Lgqa_slow%=\n"
    ".Lgqa_end%=:\n"
    :
    : "s"((unsigned)q), "s"((unsigned)(q >> 32)), "s"((unsigned)k), "s"((unsigned)(k >> 32)), "s"((unsigned)v), "s"((unsigned)(v >> 32)),
      "s"((unsigned)o), "s"((unsigned)(o >> 32)), "s"(wid), "s"(ldsb), "s"(wid * 32768u + ((wid + kv) & 3u) * 8192u)
    : "v0", "v1", "v2", "v3", "v4", "v5", "v6", "v7", "v8", "v9", "v10", "v11", "v12", "v13", "v14", "v15", "v16", "v17", "v18", "v19", "v20", "v21", "v22", "v23", "v24", "v25", "v26", "v27", "v28", "v29", "v30", "v31", "v32", "v33", "v34", "v35", "v36", "v37", "v38", "v39", "v40", "v41", "v42", "v43", "v44", "v45", "v46", "v47", "v48", "v49", "v50", "v51", "v52", "v53", "v54", "v55", "v56", "v57", "v58", "v59", "v60", "v61", "v62", "v63", "v64", "v65", "v66", "v67", "v68", "v69", "v70", "v71", "v72", "v73", "v74", "v75", "v76", "v77", "v78", "v79", "v80", "v81", "v82", "v83", "v84", "v85", "v86", "v87", "v88", "v89", "v90", "v91", "v92", "v93", "v94", "v95", "v96", "v97", "v98", "v99", "v100", "v101", "v102", "v103", "v104", "v105", "v106", "v107", "v108", "v109", "v110", "v111", "v112", "v113", "v114", "v115", "v116", "v117", "v118", "v119", "v120", "v121", "v122", "v123", "v124", "v125", "v126", "v127", "v128", "v129", "v130", "v131", "v132", "v133", "v134", "v135", "v136", "v137", "v138", "v139", "v140", "v141", "v142", "v143", "v144", "v145", "v146", "v147", "v148", "v149", "v150", "v151", "v152", "v153", "v154", "v155", "v156", "v157", "v158", "v159", "v160", "v161", "v162", "v163", "v164", "v165", "v166", "v167", "v168", "v169", "v170", "v171", "v172", "v173", "v174", "v175", "v176", "v177", "v178", "v179", "v180", "v181", "v182", "v183", "v184", "v185", "v186", "v187", "v188", "v189", "v190", "v191", "v192", "v193", "v194", "v195", "v196", "v197", "v198", "v199", "v200", "v201", "v202", "v203", "v204", "v205", "v206", "v207", "v208", "v209", "v210", "v211", "v212", "v213", "v214", "v215", "v216", "v217", "v218", "v219", "v220", "v221", "v222", "v223", "v224", "v225", "v226", "v227", "v228", "v229", "v230", "v231", "v232", "v233", "v234", "v235", "v236", "v237", "v238", "v239", "v240", "v241", "v242", "v243", "v244", "v245", "v246", "v247", "v248", "v249", "v250", "v251", "v252", "v253", "v254", "v255", "a0", "a1", "a2", "a3", "a4", "a5", "a6", "a7", "a8", "a9", "a10", "a11", "a12", "a13", "a14", "a15", "a16", "a17", "a18", "a19", "a20", "a21", "a22", "a23", "a24", "a25", "a26", "a27", "a28", "a29", "a30", "a31", "a32", "a33", "a34", "a35", "a36", "a37", "a38", "a39", "a40", "a41", "a42", "a43", "a44", "a45", "a46", "a47", "a48", "a49", "a50", "a51", "a52", "a53", "a54", "a55", "a56", "a57", "a58", "a59", "a60", "a61", "a62", "a63", "a64", "a65", "a66", "a67", "a68", "a69", "a70", "a71", "a72", "a73", "a74", "a75", "a76", "a77", "a78", "a79", "a80", "a81", "a82", "a83", "a84", "a85", "a86", "a87", "a88", "a89", "a90", "a91", "a92", "a93", "a94", "a95", "a96", "a97", "a98", "a99", "a100", "a101", "a102", "a103", "a104", "a105", "a106", "a107", "a108", "a109", "a110", "a111", "a112", "a113", "a114", "a115", "a116", "a117", "a118", "a119", "a120", "a121", "a122", "a123", "a124", "a125", "a126", "a127", "a128", "a129", "a130", "a131", "a132", "a133", "a134", "a135", "a136", "a137", "a138", "a139", "a140", "a141", "a142", "a143", "a144", "a145", "a146", "a147", "a148", "a149", "a150", "a151", "a152", "a153", "a154", "a155", "a156", "a157", "a158", "a159", "a160", "a161", "a162", "a163", "a164", "a165", "a166", "a167", "a168", "a169", "a170", "a171", "a172", "a173", "a174", "a175", "a176", "a177", "a178", "a179", "a180", "a181", "a182", "a183", "a184", "a185", "a186", "a187", "a188", "a189", "a190", "a191", "a192", "a193", "a194", "a195", "a196", "a197", "a198", "a199", "a200", "a201", "a202", "a203", "a204", "a205", "a206", "a207", "a208", "a209", "a210", "a211", "a212", "a213", "a214", "a215", "a216", "a217", "a218", "a219", "a220", "a221", "a222", "a223", "a224", "a225", "a226", "a227", "a228", "a229", "a230", "a231", "a232", "a233", "a234", "a235", "a236", "a237", "a238", "a239", "a240", "a241", "a242", "a243", "a244", "a245", "a246", "a247", "a248", "a249", "a250", "a251", "a252", "a253", "a254", "a255", "s36", "s37", "s38", "s39", "s40", "s41", "s42", "s43", "s44", "s45", "s46", "s47", "s48", "s49", "s50", "s51", "s52", "s53", "s54", "s55", "s56", "s57", "s58", "s59", "s60", "s61", "s62", "s63", "s64", "s65", "s66", "s67", "s68", "s69", "s70", "s71", "s72", "s73", "s74", "s75", "s76", "s77", "s78", "s79", "s80", "s81", "vcc", "scc", "memory");
}

extern "C" void kernel_launch(void* const* d_in, const int* in_sizes, int n_in, void* d_out, int out_size, void* d_ws, size_t ws_size,
                              hipStream_t stream) {
  (void)in_sizes; (void)n_in; (void)out_size; (void)d_ws; (void)ws_size;
  hipLaunchKernelGGL(gqa_softmax_attn_kernel, dim3(256), dim3(256), 0, stream, (const char*)d_in[0], (const char*)d_in[1],
                     (const char*)d_in[2], (char*)d_out);
}
```
